```python
import torch

def _softmax(x, axis=-1):
    return torch.softmax(x, dim=axis)
def _gelu(x):
    return torch.nn.functional.gelu(x, approximate="tanh")
def _silu(x):
    return torch.nn.functional.silu(x)
def _sigmoid(x):
    return torch.sigmoid(x)
def _layernorm(x, axis=-1, eps=1e-5):
    m = x.mean(dim=axis, keepdim=True)
    v = x.var(dim=axis, keepdim=True, unbiased=False)
    return (x - m) / torch.sqrt(v + eps)
def _scatter_add(tgt, idx, src, axis=0):
    return tgt.clone().index_add_(axis, idx, src)
def _segment_sum(src, seg, n):
    out = torch.zeros((n,) + src.shape[1:], dtype=src.dtype)
    return out.index_add_(0, seg, src)
def _pad(x, pw):
    flat = tuple(v for pair in reversed(pw) for v in pair)
    return torch.nn.functional.pad(x, flat)

kernel_name = 'interleave_layers__gqa_block_cached__glu_gated_swiglu__decode'

def setup_inputs():

    _w = (lambda a: (lambda t: t.to(torch.bfloat16) if t.is_floating_point() else t)(torch.as_tensor(a)))
    def _A_setup():
        B, Sq, D = 1, 1, 512
        Hq, Hkv, Sk, Dh = 32, 8, 65536, 128
        assert Hq % Hkv == 0, (Hq, Hkv)
        return {
            "x": _w((lambda *s: torch.randn(*s, dtype=torch.bfloat16) * 0.05)(B, Sq, D)),
            "k_cache": _w((lambda *s: torch.randn(*s, dtype=torch.bfloat16) * 0.8)(B, Hkv, Sk, Dh)),
            "v_cache": _w((lambda *s: torch.randn(*s, dtype=torch.bfloat16) * 1.6)(B, Hkv, Sk, Dh)),
            "wq": _w((lambda *s: torch.randn(*s, dtype=torch.bfloat16) * 0.8)(D, Hq * Dh) * (D ** -0.5)),
            "wo": _w((lambda *s: torch.randn(*s, dtype=torch.bfloat16) * 1.6)(Hq * Dh, D) * ((Hq * Dh) ** -0.5)),
        }
    _A_in = _A_setup()

    def _B_setup():
        B, S, D, F = 1, 1, 512, 1792
        return {
            "x": _w((lambda *s: torch.randn(*s, dtype=torch.bfloat16) * 0.05)(B, S, D)),
            "w1": _w((lambda *s: torch.randn(*s, dtype=torch.bfloat16) * 0.4)(D, F) * (2.0 / D) ** 0.5),
            "w3": _w((lambda *s: torch.randn(*s, dtype=torch.bfloat16) * 0.4)(D, F) * (2.0 / D) ** 0.5),
            "w2": _w((lambda *s: torch.randn(*s, dtype=torch.bfloat16) * 0.4)(F, D) * (2.0 / F) ** 0.5),
        }
    _B_in = _B_setup()

    return {
        "A_x": _A_in["x"],
        "A_k_cache": _A_in["k_cache"],
        "A_v_cache": _A_in["v_cache"],
        "A_wq": _A_in["wq"],
        "A_wo": _A_in["wo"],
        "B_x": _B_in["x"],
        "B_w1": _B_in["w1"],
        "B_w3": _B_in["w3"],
        "B_w2": _B_in["w2"],
    }

def reference(A_x, A_k_cache, A_v_cache, A_wq, A_wo, B_x, B_w1, B_w3, B_w2):
    def _A_ref(x, k_cache, v_cache, wq, wo):
        B, Sq, D = x.shape
        Hq, Dh = 32, 128
        Hkv = k_cache.shape[1]
        rep = Hq // Hkv
        q = (lambda x, *d: x.permute(*d))((x @ wq).reshape(B, Sq, Hq, Dh), 0, 2, 1, 3)
        k = (lambda x, n, axis: torch.repeat_interleave(x, n, dim=axis))(k_cache, rep, axis=1)
        v = (lambda x, n, axis: torch.repeat_interleave(x, n, dim=axis))(v_cache, rep, axis=1)
        scores = torch.einsum("bhqd,bhkd->bhqk", q, k) * (Dh ** -0.5)
        p = _softmax(scores, axis=-1)
        o = torch.einsum("bhqk,bhkd->bhqd", p, v)
        return (lambda x, *d: x.permute(*d))(o, 0, 2, 1, 3).reshape(B, Sq, Hq * Dh) @ wo

    def _B_ref(x, w1, w3, w2):
        gate = torch.einsum("bsd,df->bsf", x, w1)
        up = torch.einsum("bsd,df->bsf", x, w3)
        h = _silu(gate) * up
        out = torch.einsum("bsf,fd->bsd", h, w2)
        return out

    x = A_x
    x = x + _A_ref(_layernorm(x, axis=-1), A_k_cache, A_v_cache, A_wq, A_wo)
    x = x + _B_ref(_layernorm(x, axis=-1), B_w1, B_w3, B_w2)
    x = x + _A_ref(_layernorm(x, axis=-1), A_k_cache, A_v_cache, A_wq, A_wo)
    x = x + _B_ref(_layernorm(x, axis=-1), B_w1, B_w3, B_w2)
    return x
```

```cpp
#include <hip/hip_runtime.h>
#include <hip/hip_bf16.h>
#include <stdint.h>

typedef unsigned short u16;
__device__ __forceinline__ float bf2f(u16 v) { return __uint_as_float(((unsigned)v) << 16); }
__device__ __forceinline__ u16 f2bf(float f) { return __builtin_bit_cast(u16, (__bf16)f); }

constexpr int D = 512, HQ = 32, HKV = 8, SK = 65536, DH = 128, F = 1792;
constexpr int NSPLIT = 128;
constexpr int KEYS = SK / NSPLIT;

__device__ void ln_to_lds(const float* x, float* sx, float* red) {
  int t = threadIdx.x;
  float a = 0.f;
  for (int i = t; i < D; i += blockDim.x) a += x[i];
  for (int o = 32; o > 0; o >>= 1) a += __shfl_xor(a, o);
  if ((t & 63) == 0) red[t >> 6] = a;
  __syncthreads();
  float s = 0.f;
  for (int w = 0; w < (int)(blockDim.x >> 6); ++w) s += red[w];
  float mean = s / D;
  __syncthreads();
  float b = 0.f;
  for (int i = t; i < D; i += blockDim.x) { float d = x[i] - mean; b += d * d; }
  for (int o = 32; o > 0; o >>= 1) b += __shfl_xor(b, o);
  if ((t & 63) == 0) red[t >> 6] = b;
  __syncthreads();
  float v = 0.f;
  for (int w = 0; w < (int)(blockDim.x >> 6); ++w) v += red[w];
  float rs = 1.0f / sqrtf(v / D + 1e-5f);
  for (int i = t; i < D; i += blockDim.x) sx[i] = (x[i] - mean) * rs;
  __syncthreads();
}

__global__ void k_init(const u16* x, float* xf) { int t = threadIdx.x; xf[t] = bf2f(x[t]); }

__global__ void k_q(const float* xf, const u16* wq, float* q) {
  __shared__ float sx[D]; __shared__ float red[16];
  ln_to_lds(xf, sx, red);
  int c = blockIdx.x * blockDim.x + threadIdx.x;
  float a = 0.f;
  for (int i = 0; i < D; ++i) a += sx[i] * bf2f(wq[(size_t)i * (HQ * DH) + c]);
  q[c] = a;
}

__global__ void __launch_bounds__(256) k_attn(const float* q, const u16* K, const u16* V, float* pm, float* pl, float* po) {
  __shared__ float sq[4 * DH];
  __shared__ float ss[4][KEYS];
  __shared__ float sm[4], sl[4];
  int s = blockIdx.x, kvh = blockIdx.y, t = threadIdx.x;
  for (int i = t; i < 4 * DH; i += 256) sq[i] = q[kvh * 4 * DH + i];
  __syncthreads();
  const u16* Kb = K + ((size_t)kvh * SK + (size_t)s * KEYS) * DH;
  const u16* Vb = V + ((size_t)kvh * SK + (size_t)s * KEYS) * DH;
  for (int k = t; k < KEYS; k += 256) {
    float a[4] = {0, 0, 0, 0};
#pragma unroll 2
    for (int d = 0; d < DH; d += 8) {
      uint4 kk = *(const uint4*)(Kb + (size_t)k * DH + d);
      unsigned w[4] = {kk.x, kk.y, kk.z, kk.w};
      for (int j = 0; j < 4; ++j) {
        float lo = __uint_as_float(w[j] << 16), hi = __uint_as_float(w[j] & 0xffff0000u);
        for (int h = 0; h < 4; ++h) a[h] += lo * sq[h * DH + d + 2 * j] + hi * sq[h * DH + d + 2 * j + 1];
      }
    }
    for (int h = 0; h < 4; ++h) ss[h][k] = a[h] * 0.08838834764831845f;
  }
  __syncthreads();
  int w = t >> 6, lane = t & 63;
  {
    float m = -INFINITY;
    for (int k = lane; k < KEYS; k += 64) m = fmaxf(m, ss[w][k]);
    for (int o = 32; o > 0; o >>= 1) m = fmaxf(m, __shfl_xor(m, o));
    float l = 0.f;
    for (int k = lane; k < KEYS; k += 64) { float p = __expf(ss[w][k] - m); ss[w][k] = p; l += p; }
    for (int o = 32; o > 0; o >>= 1) l += __shfl_xor(l, o);
    if (lane == 0) { sm[w] = m; sl[w] = l; }
  }
  __syncthreads();
  float o0 = 0.f, o1 = 0.f;
  for (int k = 0; k < KEYS; ++k) {
    unsigned vv = *(const unsigned*)(Vb + (size_t)k * DH + 2 * lane);
    float p = ss[w][k];
    o0 += p * __uint_as_float(vv << 16);
    o1 += p * __uint_as_float(vv & 0xffff0000u);
  }
  int h = kvh * 4 + w;
  po[((size_t)h * NSPLIT + s) * DH + 2 * lane] = o0;
  po[((size_t)h * NSPLIT + s) * DH + 2 * lane + 1] = o1;
  if (lane == 0) { pm[h * NSPLIT + s] = sm[w]; pl[h * NSPLIT + s] = sl[w]; }
}

__global__ void k_comb(const float* pm, const float* pl, const float* po, float* o) {
  int h = blockIdx.x, d = threadIdx.x;
  float M = -INFINITY;
  for (int s = 0; s < NSPLIT; ++s) M = fmaxf(M, pm[h * NSPLIT + s]);
  float L = 0.f, a = 0.f;
  for (int s = 0; s < NSPLIT; ++s) {
    float w = __expf(pm[h * NSPLIT + s] - M);
    L += w * pl[h * NSPLIT + s];
    a += w * po[((size_t)h * NSPLIT + s) * DH + d];
  }
  o[h * DH + d] = a / L;
}

__global__ void k_wo(const float* o, const u16* wo, const float* xin, float* xout) {
  __shared__ float red[16][17];
  int c = blockIdx.x * 16 + (threadIdx.x & 15), kq = threadIdx.x >> 4;
  float a = 0.f;
  for (int i = kq; i < HQ * DH; i += 16) a += o[i] * bf2f(wo[(size_t)i * D + c]);
  red[kq][threadIdx.x & 15] = a;
  __syncthreads();
  if (threadIdx.x < 16) {
    float s = 0.f;
    for (int k = 0; k < 16; ++k) s += red[k][threadIdx.x];
    xout[c] = xin[c] + s;
  }
}

__global__ void k_up(const float* xf, const u16* w1, const u16* w3, float* h) {
  __shared__ float sx[D]; __shared__ float red[16];
  ln_to_lds(xf, sx, red);
  int c = blockIdx.x * blockDim.x + threadIdx.x;
  float g = 0.f, u = 0.f;
  for (int i = 0; i < D; ++i) { g += sx[i] * bf2f(w1[(size_t)i * F + c]); u += sx[i] * bf2f(w3[(size_t)i * F + c]); }
  h[c] = g / (1.0f + __expf(-g)) * u;
}

__global__ void k_down(const float* h, const u16* w2, const float* xin, float* xout, u16* out) {
  __shared__ float red[16][17];
  int c = blockIdx.x * 16 + (threadIdx.x & 15), kq = threadIdx.x >> 4;
  float a = 0.f;
  for (int i = kq; i < F; i += 16) a += h[i] * bf2f(w2[(size_t)i * D + c]);
  red[kq][threadIdx.x & 15] = a;
  __syncthreads();
  if (threadIdx.x < 16) {
    float s = 0.f;
    for (int k = 0; k < 16; ++k) s += red[k][threadIdx.x];
    float r = xin[c] + s;
    xout[c] = r;
    if (out) out[c] = f2bf(r);
  }
}

extern "C" void kernel_launch(void* const* d_in, const int* in_sizes, int n_in, void* d_out, int out_size,
                              void* d_ws, size_t ws_size, hipStream_t stream) {
  const u16* x = (const u16*)d_in[0];
  const u16* K = (const u16*)d_in[1];
  const u16* V = (const u16*)d_in[2];
  const u16* wq = (const u16*)d_in[3];
  const u16* wo = (const u16*)d_in[4];
  const u16* w1 = (const u16*)d_in[6];
  const u16* w3 = (const u16*)d_in[7];
  const u16* w2 = (const u16*)d_in[8];
  float* ws = (float*)d_ws;
  float* xa = ws;
  float* xb = ws + 512;
  float* q = ws + 1024;
  float* o = q + 4096;
  float* h = o + 4096;
  float* pm = h + 2048;
  float* pl = pm + HQ * NSPLIT;
  float* po = pl + HQ * NSPLIT;
  k_init<<<1, 512, 0, stream>>>(x, xa);
  for (int layer = 0; layer < 2; ++layer) {
    k_q<<<16, 256, 0, stream>>>(xa, wq, q);
    k_attn<<<dim3(NSPLIT, HKV), 256, 0, stream>>>(q, K, V, pm, pl, po);
    k_comb<<<32, 128, 0, stream>>>(pm, pl, po, o);
    k_wo<<<32, 256, 0, stream>>>(o, wo, xa, xb);
    k_up<<<7, 256, 0, stream>>>(xb, w1, w3, h);
    k_down<<<32, 256, 0, stream>>>(h, w2, xb, xa, layer == 1 ? (u16*)d_out : nullptr);
  }
}
```

```cpp
#include <hip/hip_runtime.h>
#include <stdint.h>

typedef unsigned u32;
typedef unsigned long long u64;
typedef unsigned short u16;
typedef u32 v4u __attribute__((ext_vector_type(4)));
typedef u32 v2u __attribute__((ext_vector_type(2)));
typedef float f32x4 __attribute__((ext_vector_type(4)));
typedef float f32x2 __attribute__((ext_vector_type(2)));
typedef __bf16 bf16x8 __attribute__((ext_vector_type(8)));
typedef __bf16 bf16x2 __attribute__((ext_vector_type(2)));
typedef __amdgpu_buffer_rsrc_t rsrc_t;

#define KV_AUX 2
#define W_AUX 0
#define RING 10

constexpr int AUX_SC1 = 16;
constexpr u32 SENT = 0xFFFFFFFFu;
constexpr int SPIN_CAP = 200000;

constexpr int OFF_EPOCH = 0;
constexpr int OFF_READY = 4096;
constexpr int OFF_Q0 = 8192;
constexpr int OFF_HO = OFF_Q0 + 32768;
constexpr int PA_REC = 1152;
constexpr int OFF_PA = OFF_HO;
constexpr int OFF_XB = OFF_PA + 2 * 16 * 32 * PA_REC;
constexpr int OFF_H = OFF_XB + 2 * 16 * 2048;
constexpr int OFF_X2 = OFF_H + 2 * 8192;
constexpr int OFF_FL = OFF_X2 + 2048;
constexpr int OFF_Q1 = OFF_FL + 2048;
constexpr int HO_USED = OFF_Q1 + 32768 - OFF_HO;
constexpr int HO_SLICE = 5120;
static_assert(HO_USED <= 256 * HO_SLICE, "reset slices must cover the hand-off region");
constexpr int OFF_STAMP = 4 << 20;
constexpr int WS_BYTES = OFF_HO + 256 * HO_SLICE;

constexpr int RED_STRIDE = 80;
constexpr int L_RED = 0;
constexpr int L_ML = L_RED + 6 * RED_STRIDE * 4;
constexpr int L_MRG = L_ML + 128;
constexpr int L_RX = L_MRG + 1024;
constexpr int L_XI = L_MRG + 8192;
constexpr int L_W2 = L_XI + 2048;
constexpr int L_WQ = L_W2 + 4096;
constexpr int L_W13 = L_WQ + 16384;
constexpr int L_V = L_W13 + 16384;
constexpr int L_BYTES = L_V + 114688;
static_assert(L_BYTES <= 163840 && L_V % 16 == 0, "LDS");

#define DLY_A 12
#define DLY_B 0
#define DLY_C1 12
#define DLY_C2 12
#define DLY_D 5
#define DLY_RETRY 3

struct Params {
  const u16* x; const u16* K; const u16* V; const u16* wq; const u16* wo;
  const u16* w1; const u16* w3; const u16* w2; u16* out; unsigned char* ws;
};

__device__ __forceinline__ float u2f(u32 x) { return __builtin_bit_cast(float, x); }
__device__ __forceinline__ u32 f2u(float x) { return __builtin_bit_cast(u32, x); }
__device__ __forceinline__ float bflo(u32 x) { return u2f(x << 16); }
__device__ __forceinline__ float bfhi(u32 x) { return u2f(x & 0xffff0000u); }
__device__ __forceinline__ u32 pack_bf16(float lo, float hi) {
  bf16x2 r = {(__bf16)lo, (__bf16)hi};
  return __builtin_bit_cast(u32, r);
}
__device__ __forceinline__ u32 canon(u32 x) { return x == SENT ? 0x7FC07FC0u : x; }
template <int CTRL> __device__ __forceinline__ float dppf(float x) {
  return u2f((u32)__builtin_amdgcn_mov_dpp((int)f2u(x), CTRL, 0xf, 0xf, true));
}
template <int CTRL> __device__ __forceinline__ u32 dppu(u32 x) {
  return (u32)__builtin_amdgcn_mov_dpp((int)x, CTRL, 0xf, 0xf, true);
}
constexpr int XOR1 = 0xB1, XOR2 = 0x4E, XOR3 = 0x1B, HMIR = 0x141, MIR = 0x140, ROR8 = 0x128;
__device__ __forceinline__ float swap32_add(float a, float b) {
  auto r = __builtin_amdgcn_permlane32_swap(f2u(a), f2u(b), false, false);
  return u2f(r[0]) + u2f(r[1]);
}
__device__ __forceinline__ float swap16_add(float a, float b) {
  auto r = __builtin_amdgcn_permlane16_swap(f2u(a), f2u(b), false, false);
  return u2f(r[0]) + u2f(r[1]);
}
__device__ __forceinline__ float xrow_max(float x) {
  auto s = __builtin_amdgcn_permlane16_swap(f2u(x), f2u(x), false, false);
  x = fmaxf(u2f(s[0]), u2f(s[1]));
  auto t = __builtin_amdgcn_permlane32_swap(f2u(x), f2u(x), false, false);
  return fmaxf(u2f(t[0]), u2f(t[1]));
}
__device__ __forceinline__ float xrow_sum(float x) {
  x = swap16_add(x, x);
  return swap32_add(x, x);
}
__device__ __forceinline__ float wave_sum(float x) {
  x += dppf<XOR1>(x); x += dppf<XOR2>(x); x += dppf<HMIR>(x); x += dppf<MIR>(x);
  return xrow_sum(x);
}
__device__ __forceinline__ float treduce16(float (&v)[16], int lane) {
#pragma unroll
  for (int i = 0; i < 8; ++i) v[i] = swap32_add(v[i], v[i + 8]);
#pragma unroll
  for (int i = 0; i < 4; ++i) v[i] = swap16_add(v[i], v[i + 4]);
  const bool b3 = lane & 8, b2 = lane & 4;
#pragma unroll
  for (int i = 0; i < 2; ++i) {
    const float keep = b3 ? v[i + 2] : v[i], send = b3 ? v[i] : v[i + 2];
    v[i] = keep + dppf<ROR8>(send);
  }
  const float keep = b2 ? v[1] : v[0], send = b2 ? v[0] : v[1];
  float r = keep + dppf<HMIR>(send);
  r += dppf<XOR2>(r);
  r += dppf<XOR1>(r);
  return r;
}
__device__ __forceinline__ float treduce8(float (&v)[8], int lane) {
#pragma unroll
  for (int i = 0; i < 4; ++i) v[i] = swap32_add(v[i], v[i + 4]);
#pragma unroll
  for (int i = 0; i < 2; ++i) v[i] = swap16_add(v[i], v[i + 2]);
  const bool b3 = lane & 8;
  const float keep = b3 ? v[1] : v[0], send = b3 ? v[0] : v[1];
  float r = keep + dppf<ROR8>(send);
  r += dppf<XOR1>(r);
  r += dppf<XOR2>(r);
  r += dppf<HMIR>(r);
  return r;
}
__device__ __forceinline__ f32x4 mfma(v4u a, v4u b, f32x4 c) {
  return __builtin_amdgcn_mfma_f32_16x16x32_bf16(__builtin_bit_cast(bf16x8, a), __builtin_bit_cast(bf16x8, b), c, 0, 0, 0);
}
__device__ __forceinline__ f32x4 qk1(v4u k, v4u q) {
  f32x4 d;
  asm volatile("v_mfma_f32_16x16x32_bf16 %0, %1, %2, 0" : "=&v"(d) : "v"(k), "v"(q));
  return d;
}
__device__ __forceinline__ void qk_fence(f32x4 (&D)[4]) {
  asm volatile("s_nop 7" : "+v"(D[0]), "+v"(D[1]), "+v"(D[2]), "+v"(D[3]));
}
__device__ __forceinline__ void pv2a(f32x4& a0, f32x4& a1, v4u va, v4u vb, v4u pv) {
  asm volatile("s_nop 1\n\t"
      "v_mfma_f32_16x16x32_bf16 %0, %2, %4, %0\n\t"
      "v_mfma_f32_16x16x32_bf16 %1, %3, %4, %1"
      : "+v"(a0), "+v"(a1) : "a"(va), "a"(vb), "v"(pv));
}
__device__ __forceinline__ void pv2v(f32x4& a0, f32x4& a1, v4u va, v4u vb, v4u pv) {
  asm volatile("s_nop 1\n\t"
      "v_mfma_f32_16x16x32_bf16 %0, %2, %4, %0\n\t"
      "v_mfma_f32_16x16x32_bf16 %1, %3, %4, %1"
      : "+v"(a0), "+v"(a1) : "v"(va), "v"(vb), "v"(pv));
}
__device__ __forceinline__ v4u ldw(const void* base, u32 off) {
  const v4u* q = (const v4u*)((const unsigned char*)base + off);
  return *q;
}
constexpr int home(int T, int es) { return T >= 12 ? 2 : T == 11 ? (es < 2 ? 1 : 2) : (T % 3 == 2 ? 1 : 0); }
constexpr int hidx(int T, int es) { return T >= 12 ? T - 12 : T == 11 ? (es < 2 ? 3 : 4) : (T % 3 == 2 ? T / 3 : T - T / 3); }
constexpr int lslot(int T, int e) { return T == 11 ? 24 + (e >> 2) * 2 + (e & 3) : (T / 3) * 8 + e; }
template <int AUX> __device__ __forceinline__ v4u ld128(rsrc_t rs, int voff, int soff) {
  return __builtin_amdgcn_raw_buffer_load_b128(rs, voff, soff, AUX);
}
#define KEEPALL 0
#define ROTMUL 5
#define ROTMUL2 16
#define L2MOD 5
#define L2MASK 4
__device__ __forceinline__ int chunk(int j, int rot4k) { return (j * 4096 + rot4k) & 0x7FFFF; }
__device__ __forceinline__ v4u ldk(rsrc_t rs, int voff, int j, int rot4k, bool keep = false) {
  if (L2MOD && j >= RING && ((L2MASK >> (j % (L2MOD ? L2MOD : 1))) & 1) && keep) return ld128<0>(rs, voff, chunk(j, rot4k));
  return ld128<KV_AUX>(rs, voff, chunk(j, rot4k));
}
__device__ __forceinline__ int qoff(int voffK) { return ((voffK >> 5) & 31) * 128 + (voffK & 31); }
__device__ __forceinline__ u32 umax(u32 a, u32 b) { return a > b ? a : b; }
__device__ __forceinline__ u32 max4(v4u v) { return umax(umax(v.x, v.y), umax(v.z, v.w)); }
#define BAR() asm volatile("s_waitcnt lgkmcnt(0)\n\ts_barrier" ::: "memory")
#define CLOBBER() asm volatile("" ::: "memory")
#define SCHED() __builtin_amdgcn_sched_barrier(0)
#define TILEBAR 1
#define PACE_OFF1 300
#define PACE_OFF2 2300
#define PACE_OFF3 -120
#define PACE2 770
#define PACE1 0
#define PACE 920
__device__ __forceinline__ void pace(u32 t0, int due) {
  if (PACE) {
    asm volatile("1:\n\ts_memrealtime vcc\n\ts_waitcnt lgkmcnt(0)\n\ts_sub_u32 vcc_lo, vcc_lo, %0\n\t"
                 "s_cmp_lt_i32 vcc_lo, %1\n\ts_cbranch_scc1 1b" ::"s"(t0), "s"(due) : "vcc", "scc", "memory");
  }
  if (TILEBAR) __builtin_amdgcn_s_barrier();
}

#define STAMP(i) do { } while (0)

__device__ __forceinline__ void fma8(float* acc, float x, v4u w) {
  acc[0] = fmaf(x, bflo(w.x), acc[0]); acc[1] = fmaf(x, bfhi(w.x), acc[1]);
  acc[2] = fmaf(x, bflo(w.y), acc[2]); acc[3] = fmaf(x, bfhi(w.y), acc[3]);
  acc[4] = fmaf(x, bflo(w.z), acc[4]); acc[5] = fmaf(x, bfhi(w.z), acc[5]);
  acc[6] = fmaf(x, bflo(w.w), acc[6]); acc[7] = fmaf(x, bfhi(w.w), acc[7]);
}

__device__ __forceinline__ void unpack_w(const v4u (&wt)[4], f32x2 (&wf)[2][8]) {
#pragma unroll
  for (int r = 0; r < 2; ++r)
#pragma unroll
    for (int i = 0; i < 4; ++i) {
      wf[r][i] = f32x2{bflo(wt[r][i]), bfhi(wt[r][i])};
      wf[r][4 + i] = f32x2{bflo(wt[2 + r][i]), bfhi(wt[2 + r][i])};
    }
#pragma unroll
  for (int r = 0; r < 2; ++r)
#pragma unroll
    for (int i = 0; i < 8; ++i) asm volatile("" : "+v"(wf[r][i]));
}
__device__ __forceinline__ void ln_gemv_part(float da, float db, const f32x2 (&wf)[2][8], float* red, int w, int lane) {
  float a[16];
#pragma unroll
  for (int i = 0; i < 8; ++i) {
    const f32x2 t = da * wf[0][i] + db * wf[1][i];
    a[2 * i] = t.x; a[2 * i + 1] = t.y;
  }
  float st = swap32_add(da + db, fmaf(da, da, db * db));
  st = swap16_add(st, st);
  st += dppf<XOR1>(st); st += dppf<XOR2>(st); st += dppf<HMIR>(st); st += dppf<MIR>(st);
  const float r = treduce16(a, lane);
  const bool isr = (lane & 3) == 0;
  const int slot = isr ? (lane >> 2) : 16 + (lane >> 5);
  if (isr || (lane & 31) == 1) red[slot * 4 + w] = isr ? r : st;
}
__device__ __forceinline__ float sum4(f32x4 v) { return (v.x + v.y) + (v.z + v.w); }
__device__ __forceinline__ float ln_gemv_final(const float* red, float cs, int lane, float& mu) {
  const f32x4 g = *(const f32x4*)(red + (lane & 15) * 4), s1 = *(const f32x4*)(red + 64), s2 = *(const f32x4*)(red + 68);
  mu = sum4(s1) * (1.0f / 512.0f);
  const float var = fmaf(-mu, mu, sum4(s2) * (1.0f / 512.0f));
  return fmaf(-mu, cs, sum4(g)) * __builtin_amdgcn_rsqf(fmaxf(var, 0.f) + 1e-5f);
}

template <int I> struct IC { static constexpr int value = I; };
template <int I, int N, class F> __device__ __forceinline__ void static_for(F&& f) {
  if constexpr (I < N) { f(IC<I>{}); static_for<I + 1, N>(f); }
}

__global__ void __launch_bounds__(256, 1) decode_kernel(Params p) {
  __shared__ __attribute__((aligned(16))) unsigned char smem[L_BYTES];
  const int tid = threadIdx.x, lane = tid & 63, w = __builtin_amdgcn_readfirstlane(tid >> 6);
  const int b = blockIdx.x, kvh = b & 7, sp = b >> 3;
  u32 tp = PACE ? (u32)__builtin_amdgcn_s_memrealtime() : 0u;
  const rsrc_t rsW = __builtin_amdgcn_make_buffer_rsrc((void*)p.ws, 0, WS_BYTES, 0x00020000);
  const size_t kvoff = ((size_t)kvh * 65536 + (size_t)sp * 2048) * 128;
  const rsrc_t rsK = __builtin_amdgcn_make_buffer_rsrc((void*)(p.K + kvoff), 0, 2048 * 256, 0x00020000);
  const rsrc_t rsV = __builtin_amdgcn_make_buffer_rsrc((void*)(p.V + kvoff), 0, 2048 * 256, 0x00020000);

  float* const red = (float*)(smem + L_RED);

  int rot4k = ((sp * ROTMUL + kvh * ROTMUL2) & 127) * 4096;
  const int voffV = lane * 16 + w * 1024;
  const int voffK = ((lane >> 2) & 3) * 256 + ((lane >> 4) * 4 + (lane & 3)) * 16 + w * 1024;
  v4u vresA[8][8], vresV[5][8];
  v4u raw[3][8];
  v4u kb[RING];
#pragma unroll
  for (int T = 0; T < 3; ++T)
#pragma unroll
    for (int t = 0; t < 8; ++t) raw[T][t] = ld128<KV_AUX>(rsV, voffV, chunk(8 * T + t, rot4k));
  SCHED();
  u32 touch[6];
  touch[0] = *(const u32*)((const unsigned char*)p.wq + (u32)((16 * sp + 4 * w + (lane >> 4)) * 8192 + kvh * 1024 + (lane & 15) * 64));
  if (w == 0) {
    const v4u s4 = {SENT, SENT, SENT, SENT};
#pragma unroll
    for (int i = 0; i < 5; ++i) __builtin_amdgcn_raw_buffer_store_b128(s4, rsW, OFF_HO + lane * 16 + i * 1024, b * HO_SLICE, AUX_SC1);
  }
  const v2u ep = __builtin_amdgcn_raw_buffer_load_b64(rsW, OFF_EPOCH, 0, AUX_SC1);
  const int row0 = w * 128 + 4 * (lane & 31) + 2 * (lane >> 5);
  const int qcol = kvh * 512 + sp * 16;
  const int rg = 2 * kvh + (sp & 1), cs = sp >> 1;
  const bool doS3 = sp < 28;
  const int s3 = kvh * 28 + (doS3 ? sp : 0);
  const int dcol = kvh * 64 + sp * 2;
  u32 x01 = 0, x00 = 0;
  SCHED();

  float cmean = 0.f;
  u32 tag_lo = 0, tag_hi = 0;
  u32 w2r[7];
  v4u qf = {0u, 0u, 0u, 0u}, r0 = qf, r1 = qf;
  v4u wtmp[8];
  v4u* const ldsV = (v4u*)(smem + L_V) + w * (28 * 64) + lane;
  v4u* const ldsWq = (v4u*)(smem + L_WQ);
  v4u* const ldsW13 = (v4u*)(smem + L_W13);
  static_for<0, 16>([&](auto TC) __attribute__((always_inline)) {
    constexpr int T = decltype(TC)::value;
    v4u f[8];
#pragma unroll
    for (int e = 0; e < 8; ++e)
#pragma unroll
      for (int j = 0; j < 4; ++j)
        f[e][j] = __builtin_amdgcn_perm(raw[T % 3][j + 4][e >> 1], raw[T % 3][j][e >> 1], (e & 1) ? 0x07060302u : 0x05040100u);
#pragma unroll
    for (int e = 0; e < 8; ++e)
      if (home(T, e & 3) == 2) asm volatile("" : "+v"(f[e]));
    SCHED();
    pace(tp, T * PACE1 / 8 + PACE_OFF1);
    SCHED();
    if constexpr (T + 3 < 16) {
#pragma unroll
      for (int t = 0; t < 8; ++t) raw[T % 3][t] = ld128<KV_AUX>(rsV, voffV, chunk(8 * (T + 3) + t, rot4k));
    }
    if constexpr (T >= 13) {
#pragma unroll
      for (int i = (T - 13) * 4; i < (T == 15 ? RING : (T - 12) * 4); ++i) kb[i] = ldk(rsK, voffK, i, rot4k);
    }
    if constexpr (T == 0) {
      const int l7 = lane < 56 ? lane : 55;
      touch[1] = *(const u32*)((const unsigned char*)(w < 2 ? p.w1 : p.w3) + (u32)((16 * sp + 8 * (w & 1) + l7 / 7) * 3584 + kvh * 448 + (l7 % 7) * 64));
    }
    if constexpr (T == 1) {
      x01 = *(const u32*)(p.x + row0);
      x00 = *(const u32*)p.x;
      const int l2 = lane < 28 ? lane : 27;
      touch[2] = *(const u32*)((const unsigned char*)p.w2 + (u32)((56 * sp + 14 * w + (l2 >> 1)) * 1024 + kvh * 128 + (l2 & 1) * 64));
    }
    if constexpr (T == 2 || T == 3) {
#pragma unroll
      for (int i = 2 * (T - 2); i < 2 * (T - 2) + 2; ++i) wtmp[i] = ldw(p.wq, (w * 128 + 32 * i + (lane >> 1)) * 8192 + qcol * 2 + (lane & 1) * 16);
    }
    if constexpr (T >= 4 && T < 8) wtmp[T] = ldw(T < 6 ? p.w1 : p.w3, (row0 + (T & 1)) * 3584 + s3 * 16);
    if constexpr (T >= 6 && T < 13) w2r[T - 6] = *(const u32*)((const unsigned char*)p.w2 + (u32)((tid + 256 * (T - 6)) * 1024 + dcol * 2));
    if constexpr (T == 4) touch[3] = *(const u32*)((const unsigned char*)p.wo + (u32)((512 * kvh + 16 * sp) * 1024 + tid * 64));
    if constexpr (T == 5) touch[4] = __builtin_amdgcn_raw_buffer_load_b32(rsK, lane * 8192, 0, KV_AUX);
    if constexpr (T == 9) touch[5] = *(const u32*)(p.out + 2 * lane);
    if constexpr (T >= 9 && T < 13) ((u32*)(smem + L_W2))[(T - 9) * 256 + tid] = w2r[T - 9];
    if constexpr (T == 5) asm volatile("" ::"v"(touch[0]), "v"(touch[1]), "v"(touch[2]));
    if constexpr (T == 13) asm volatile("" ::"v"(touch[3]), "v"(touch[4]), "v"(touch[5]));
#pragma unroll
    for (int e = 0; e < 8; ++e) {
      if (home(T, e & 3) == 1) ldsV[lslot(T, e) * 64] = f[e];
      else if (home(T, e & 3) == 2) vresV[hidx(T, e & 3)][e] = f[e];
      else { asm volatile("" : "+a"(f[e])); vresA[hidx(T, e & 3)][e] = f[e]; }
    }
    SCHED();
    if constexpr (T == 0) STAMP(24);
    if constexpr (T == 6) {
#pragma unroll
      for (int i = 0; i < 4; ++i) {
        const int rho = 32 * i + (lane >> 1);
        ldsWq[((lane & 1) * 2 + (rho & 1)) * 256 + w * 64 + (rho >> 2) + 32 * ((rho >> 1) & 1)] = wtmp[i];
      }
      v4u wt[4];
#pragma unroll
      for (int i = 0; i < 4; ++i) wt[i] = ldsWq[i * 256 + tid];
      f32x2 wf[2][8];
      unpack_w(wt, wf);
      const float xia = bflo(x01), xib = bfhi(x01);
      ((f32x2*)(smem + L_XI))[tid] = f32x2{xia, xib};
      cmean = bflo(x00);
      ln_gemv_part(1.0f, 1.0f, wf, red + 4 * RED_STRIDE, w, lane);
      ln_gemv_part(xia - cmean, xib - cmean, wf, red, w, lane);
      BAR();
      const float csq = sum4(*(const f32x4*)(red + 4 * RED_STRIDE + (lane & 15) * 4));
      float mu;
      const float qv = ln_gemv_final(red, csq, lane, mu);
      cmean = u2f((u32)__builtin_amdgcn_readfirstlane((int)f2u(cmean + mu)));
      const u32 pk = canon(pack_bf16(qv, dppf<XOR1>(qv)));
      if (w == 0 && lane < 16 && (lane & 1) == 0) __builtin_amdgcn_raw_buffer_store_b32(pk, rsW, lane * 2, OFF_Q0 + qcol * 8, AUX_SC1);
      const u64 epoch = (((u64)ep.y << 32) | ep.x) + 0x9E3779B97F4A7C15ull;
      tag_lo = (u32)__builtin_amdgcn_readfirstlane((int)(u32)epoch);
      tag_hi = (u32)__builtin_amdgcn_readfirstlane((int)(u32)(epoch >> 32));
      STAMP(1);
      SCHED();
    }
    if constexpr (T == 8) if (w == 0) {
      asm volatile("s_waitcnt vmcnt(16)" ::: "memory");
      if (lane == 0) __builtin_amdgcn_raw_buffer_store_b64(v2u{tag_lo, tag_hi}, rsW, 0, OFF_READY + b * 8, AUX_SC1);
      SCHED();
    }
    if constexpr (T == 10) {
      const v4u wt[4] = {wtmp[4], wtmp[5], wtmp[6], wtmp[7]};
#pragma unroll
      for (int i = 0; i < 4; ++i) ldsW13[i * 256 + tid] = wt[i];
      f32x2 wf[2][8];
      unpack_w(wt, wf);
      ln_gemv_part(1.0f, 1.0f, wf, red + 5 * RED_STRIDE, w, lane);
      SCHED();
    }
    if constexpr (T == 12) {
      r0 = ld128<AUX_SC1>(rsW, OFF_READY + lane * 16, 0);
      r1 = ld128<AUX_SC1>(rsW, OFF_READY + 1024 + lane * 16, 0);
      SCHED();
    }
    if constexpr (T == 14) {
      auto not_ready = [&](v4u a, v4u c) {
        const u32 bad = ((a.x ^ tag_lo) | (a.y ^ tag_hi) | (a.z ^ tag_lo) | (a.w ^ tag_hi)) |
                        ((c.x ^ tag_lo) | (c.y ^ tag_hi) | (c.z ^ tag_lo) | (c.w ^ tag_hi));
        return __any(bad != 0);
      };
      if (__builtin_expect(not_ready(r0, r1), 0)) {
_Pragma("unroll 1")
        for (int spin = 0; spin < SPIN_CAP; ++spin) {
          __builtin_amdgcn_s_sleep(8);
          CLOBBER();
          const v4u a = ld128<AUX_SC1>(rsW, OFF_READY + lane * 16, 0), c = ld128<AUX_SC1>(rsW, OFF_READY + 1024 + lane * 16, 0);
          if (!not_ready(a, c)) break;
        }
      }
      CLOBBER();
      qf = ld128<AUX_SC1>(rsW, qoff(voffK), OFF_Q0 + kvh * 4096);
      if (b == 0 && tid == 0) __builtin_amdgcn_raw_buffer_store_b64(v2u{tag_lo, tag_hi}, rsW, OFF_EPOCH, 0, AUX_SC1);
      SCHED();
    }
  });
  STAMP(2);

  const int c4 = lane & 3;
  const bool cb0 = lane & 1, cb1 = lane & 2;
  constexpr float C2 = 0.08838834764831845f * 1.4426950408889634f;

#define PREFETCH(part) do { SCHED(); \
    if (layer == 0) { _Pragma("unroll") for (int i = (part) * 4; i < ((part) == 2 ? RING : ((part) + 1) * 4); ++i) kb[i] = ldk(rsK, voffK, i, rot4k); } \
    else { _Pragma("unroll") for (int i = (part) * 4; i < ((part) == 2 ? RING : ((part) + 1) * 4); ++i) kb[i] = v4u{0u, 0u, 0u, 0u}; } \
    SCHED(); } while (0)
#pragma unroll 1
  for (int layer = 0; layer < 2; ++layer) {
    STAMP(3 + 12 * layer);
    if (PACE && layer) tp = (u32)__builtin_amdgcn_s_memrealtime();
    const int tbase = layer ? PACE_OFF3 : PACE_OFF2, tstep = layer ? PACE2 / 8 : PACE / 8;
    float m = -INFINITY, mc = 0.f, l = 0.f;
    f32x4 acc[2] = {f32x4{0.f, 0.f, 0.f, 0.f}, f32x4{0.f, 0.f, 0.f, 0.f}};
    v4u wot[4];
    int ln = lane;
    asm volatile("s_nop 1" : "+v"(qf), "+s"(rot4k));
    static_for<0, 16>([&](auto TC) __attribute__((always_inline)) {
      constexpr int T = decltype(TC)::value;
      if constexpr (T > 0 && T < 15) { pace(tp, T * tstep + tbase); SCHED(); }
      float X[2];
#pragma unroll
      for (int hf = 0; hf < 2; ++hf) {
        f32x4 D[4];
        const int blk = 8 * T + 4 * hf;
#pragma unroll
        for (int t = 0; t < 4; ++t) {
          D[t] = qk1(kb[(blk + t) % RING], qf);
          if (blk + t + RING < 128) kb[(blk + t) % RING] = ldk(rsK, voffK, (blk + t + RING), rot4k, KEEPALL || layer == 0);
          SCHED();
        }
        if (blk == (128 - RING + 3) / 4 * 4) {
          asm volatile("" : "+v"(ln));
          const int pp = 16 * w + (ln >> 2);
          const int orow = rg * 256 + ((pp >> 2) & 1) * 128 + ((pp >> 3) & 3) * 32 + (pp >> 5) * 4 + (pp & 3);
#pragma unroll
          for (int r = 0; r < 4; ++r) wot[r] = ldw(p.wo, (orow + 8 * r) * 1024 + cs * 64 + (ln & 3) * 16);
        }
        qk_fence(D);
        SCHED();
        const float e0 = cb1 ? (cb0 ? D[3][3] : D[2][2]) : (cb0 ? D[1][1] : D[0][0]);
        const float e1 = cb1 ? (cb0 ? D[2][3] : D[3][2]) : (cb0 ? D[0][1] : D[1][0]);
        const float e2 = cb1 ? (cb0 ? D[1][3] : D[0][2]) : (cb0 ? D[3][1] : D[2][0]);
        const float e3 = cb1 ? (cb0 ? D[0][3] : D[1][2]) : (cb0 ? D[2][1] : D[3][0]);
        X[hf] = (e0 + dppf<XOR1>(e1)) + (dppf<XOR2>(e2) + dppf<XOR3>(e3));
      }
      float mx = fmaxf(X[0], X[1]);
      if (__any(mx > m)) {
        mx = fmaxf(mx, dppf<XOR1>(mx));
        mx = fmaxf(mx, dppf<XOR2>(mx));
        mx = xrow_max(mx);
        const float mn = fmaxf(m, mx);
        const float mcn = (mn == -INFINITY) ? 0.f : mn * C2;
        const float alpha = __builtin_amdgcn_exp2f(m * C2 - mcn);
        l *= alpha;
        acc[0] *= alpha; acc[1] *= alpha;
        m = mn; mc = mcn;
      }
      const float p0 = __builtin_amdgcn_exp2f(fmaf(X[0], C2, -mc)), p1 = __builtin_amdgcn_exp2f(fmaf(X[1], C2, -mc));
      l += p0 + p1;
      const u32 Y = pack_bf16(p0, p1);
      const v4u Yb = {dppu<0x00>(Y), dppu<0x55>(Y), dppu<0xAA>(Y), dppu<0xFF>(Y)};
#pragma unroll
      for (int es = 0; es < 4; ++es) {
        const bool on = c4 == es;
        const v4u pv = {on ? Yb.x : 0u, on ? Yb.y : 0u, on ? Yb.z : 0u, on ? Yb.w : 0u};
        if (home(T, es) == 0) pv2a(acc[0], acc[1], vresA[hidx(T, es)][es], vresA[hidx(T, es)][4 + es], pv);
        else if (home(T, es) == 2) pv2v(acc[0], acc[1], vresV[hidx(T, es)][es], vresV[hidx(T, es)][4 + es], pv);
        else pv2v(acc[0], acc[1], ldsV[lslot(T, es) * 64], ldsV[lslot(T, 4 + es) * 64], pv);
      }
      SCHED();
    });
    STAMP(4 + 12 * layer);

    l += dppf<XOR1>(l); l += dppf<XOR2>(l); l = xrow_sum(l);
    asm volatile("s_nop 7" : "+v"(acc[0]), "+v"(acc[1]));
    {
      f32x4* mrg = (f32x4*)(smem + L_MRG);
      mrg[(w * 2 + 0) * 64 + ln] = acc[0];
      mrg[(w * 2 + 1) * 64 + ln] = acc[1];
      f32x2* ml = (f32x2*)(smem + L_ML);
      if (ln < 16 && (ln & 3) == 0) ml[(ln >> 2) * 4 + w] = f32x2{mc, l};
      BAR();
      if (w < 2) {
        const int h = (ln >> 2) & 3;
        const f32x4 qa = *(const f32x4*)(ml + h * 4), qb = *(const f32x4*)(ml + h * 4 + 2);
        const float M = fmaxf(fmaxf(qa.x, qa.z), fmaxf(qb.x, qb.z));
        const float f0 = __builtin_amdgcn_exp2f(qa.x - M), f1 = __builtin_amdgcn_exp2f(qa.z - M);
        const float f2 = __builtin_amdgcn_exp2f(qb.x - M), f3 = __builtin_amdgcn_exp2f(qb.z - M);
        const f32x4 o = (f0 * mrg[w * 64 + ln] + f1 * mrg[(2 + w) * 64 + ln]) + (f2 * mrg[(4 + w) * 64 + ln] + f3 * mrg[(6 + w) * 64 + ln]);
        const float L = fmaf(f0, qa.y, f1 * qa.w) + fmaf(f2, qb.y, f3 * qb.w);
        const int pos = (w * 32 + (ln >> 4) * 8 + (h & 1) * 4 + (ln & 3)) * 16 + (h >> 1) * (32 * PA_REC);
        const int rec = OFF_PA + ((layer * 16 + 2 * kvh) * 32 + sp) * PA_REC;
        const v4u ov = {canon(f2u(o.x)), canon(f2u(o.y)), canon(f2u(o.z)), canon(f2u(o.w))};
        __builtin_amdgcn_raw_buffer_store_b128(ov, rsW, pos, rec, AUX_SC1);
        if (w == 0 && ln < 16 && (ln & 3) == 0)
          __builtin_amdgcn_raw_buffer_store_b64(v2u{canon(f2u(M)), canon(f2u(L))}, rsW, 1024 + (h & 1) * 8 + (h >> 1) * (32 * PA_REC), rec, AUX_SC1);
      }
    }
    STAMP(5 + 12 * layer);

    {
      if (w >= 2) __builtin_amdgcn_s_sleep(8);
      if (DLY_A) __builtin_amdgcn_s_sleep(DLY_A);
      v4u pa[8];
      v2u mlv;
      const int base = OFF_PA + (layer * 16 + rg) * 32 * PA_REC;
      CLOBBER();
      mlv = __builtin_amdgcn_raw_buffer_load_b64(rsW, 1024 + (ln >> 1) * PA_REC + (ln & 1) * 8, base, AUX_SC1);
#pragma unroll
      for (int k = 0; k < 8; ++k) pa[k] = ld128<AUX_SC1>(rsW, ln * 16, base + (8 * w + k) * PA_REC);
      SCHED();
      f32x2 wof[4][4];
#pragma unroll
      for (int r = 0; r < 4; ++r)
#pragma unroll
        for (int i = 0; i < 4; ++i) { wof[r][i] = f32x2{bflo(wot[r][i]), bfhi(wot[r][i])}; asm volatile("" : "+v"(wof[r][i])); }
      SCHED();
_Pragma("unroll 1")
      for (int spin = 0; spin < SPIN_CAP; ++spin) {
        u32 mk[8], mxw = umax(mlv.x, mlv.y);
#pragma unroll
        for (int k = 0; k < 8; ++k) { mk[k] = max4(pa[k]); mxw = umax(mxw, mk[k]); }
        if (__all(mxw != SENT)) break;
        u32 need = __any(umax(mlv.x, mlv.y) == SENT) ? 0x100u : 0u;
#pragma unroll
        for (int k = 0; k < 8; ++k) need |= __any(mk[k] == SENT) ? (1u << k) : 0u;
        CLOBBER();
        if (need & 0x100) mlv = __builtin_amdgcn_raw_buffer_load_b64(rsW, 1024 + (ln >> 1) * PA_REC + (ln & 1) * 8, base, AUX_SC1);
#pragma unroll
        for (int k = 0; k < 8; ++k)
          if (need & (1 << k)) pa[k] = ld128<AUX_SC1>(rsW, ln * 16, base + (8 * w + k) * PA_REC);
        SCHED();
      }
      if (sp == 0 && w == 0 && ln == 0) __builtin_amdgcn_raw_buffer_store_b32(1u, rsW, 0, OFF_FL + (layer * 8 + kvh) * 128, AUX_SC1);
      STAMP(6 + 12 * layer);
      const float mm = u2f(mlv.x);
      float M = fmaxf(mm, dppf<XOR2>(mm));
      M = fmaxf(M, dppf<0x124>(M));
      M = fmaxf(M, dppf<ROR8>(M));
      M = xrow_max(M);
      const float wgt = __builtin_amdgcn_exp2f(mm - M);
      float L = wgt * u2f(mlv.y);
      L += dppf<XOR2>(L);
      L += dppf<0x124>(L);
      L += dppf<ROR8>(L);
      L = xrow_sum(L);
      const float fac = wgt * __builtin_amdgcn_rcpf(L);
      f32x4 o = {0.f, 0.f, 0.f, 0.f};
      const int src = (16 * w + ((ln >> 2) & 1)) * 4;
#pragma unroll
      for (int k = 0; k < 8; ++k) {
        const float f = u2f((u32)__builtin_amdgcn_ds_bpermute(src + 8 * k, (int)f2u(fac)));
        o += f * __builtin_bit_cast(f32x4, pa[k]);
      }
      f32x4* op = (f32x4*)(smem + L_MRG);
      op[w * 128 + ln] = o;
      STAMP(27 + layer);
      BAR();
      STAMP(29 + layer);
      const int pp = 16 * w + (ln >> 2);
      o = (op[pp] + op[128 + pp]) + (op[256 + pp] + op[384 + pp]);
      float a[8];
#pragma unroll
      for (int i = 0; i < 4; ++i) {
        const f32x2 t = (o.x * wof[0][i] + o.y * wof[1][i]) + (o.z * wof[2][i] + o.w * wof[3][i]);
        a[2 * i] = t.x; a[2 * i + 1] = t.y;
      }
#pragma unroll
      for (int i = 0; i < 4; ++i) a[i] = swap32_add(a[i], a[i + 4]);
#pragma unroll
      for (int i = 0; i < 2; ++i) {
        a[i] = swap16_add(a[i], a[i + 2]);
        a[i] += dppf<ROR8>(a[i]);
        a[i] += dppf<0x124>(a[i]);
      }
      float* const rx = (float*)(smem + L_RX);
      if ((ln & 12) == 0) {
        const int col = 8 * (ln & 3) + 4 * (ln >> 5) + 2 * ((ln >> 4) & 1);
        rx[col * 4 + w] = a[0];
        rx[col * 4 + 4 + w] = a[1];
      }
      BAR();
      if (w == 0 && ln < 32)
        __builtin_amdgcn_raw_buffer_store_b32(canon(f2u(sum4(*(const f32x4*)(rx + ln * 4)))), rsW, ln * 4, OFF_XB + (layer * 16 + rg) * 2048 + cs * 128, AUX_SC1);
    }
    STAMP(7 + 12 * layer);

    {
      v4u wt[4];
#pragma unroll
      for (int i = 0; i < 4; ++i) wt[i] = ldsW13[i * 256 + w * 64 + ln];
      f32x2 wf[2][8];
      unpack_w(wt, wf);
      float cs13 = sum4(*(const f32x4*)(red + 5 * RED_STRIDE + (ln & 15) * 4));
      f32x2 xi = ((const f32x2*)(smem + L_XI))[w * 64 + ln];
      asm volatile("" : "+v"(cs13), "+v"(xi));
      if (DLY_B) __builtin_amdgcn_s_sleep(DLY_B);
_Pragma("unroll 1")
      for (int spin = 0; spin < SPIN_CAP; ++spin) {
        CLOBBER();
        const u32 fl = __builtin_amdgcn_raw_buffer_load_b32(rsW, (ln & 7) * 128, OFF_FL + layer * 1024, AUX_SC1);
        if (__all(fl != SENT || (ln & 7) == kvh)) break;
      }
      v4u g[8];
_Pragma("unroll 1")
      for (int spin = 0; spin < SPIN_CAP; ++spin) {
        CLOBBER();
#pragma unroll
        for (int k = 0; k < 8; ++k) g[k] = ld128<AUX_SC1>(rsW, (ln & 31) * 16 + (ln >> 5) * 2048, OFF_XB + layer * 32768 + w * 512 + k * 4096);
        u32 mxw = 0;
#pragma unroll
        for (int k = 0; k < 8; ++k) mxw = umax(mxw, max4(g[k]));
        if (__all(mxw != SENT)) break;
        __builtin_amdgcn_s_sleep(DLY_RETRY);
      }
      STAMP(8 + 12 * layer);
      const f32x4 sm = ((__builtin_bit_cast(f32x4, g[0]) + __builtin_bit_cast(f32x4, g[1])) + (__builtin_bit_cast(f32x4, g[2]) + __builtin_bit_cast(f32x4, g[3]))) +
                       ((__builtin_bit_cast(f32x4, g[4]) + __builtin_bit_cast(f32x4, g[5])) + (__builtin_bit_cast(f32x4, g[6]) + __builtin_bit_cast(f32x4, g[7])));
      const float x1a = xi.x + swap32_add(sm.x, sm.z), x1b = xi.y + swap32_add(sm.y, sm.w);
      ln_gemv_part(x1a - cmean, x1b - cmean, wf, red + RED_STRIDE, w, ln);
      if (w * 128 + 4 * (ln & 31) + 2 * (ln >> 5) == dcol) *(f32x2*)(red + 3 * RED_STRIDE) = f32x2{x1a, x1b};
      BAR();
      float mu;
      const float gt = ln_gemv_final(red + RED_STRIDE, cs13, ln, mu);
      const float up = dppf<ROR8>(gt);
      const float hval = gt * __builtin_amdgcn_rcpf(1.0f + __expf(-gt)) * up;
      if (doS3 && w == 0 && ln < 8) __builtin_amdgcn_raw_buffer_store_b32(canon(f2u(hval)), rsW, ln * 4, OFF_H + layer * 8192 + s3 * 32, AUX_SC1);
      cmean = u2f((u32)__builtin_amdgcn_readfirstlane((int)f2u(cmean + mu)));
    }
    STAMP(9 + 12 * layer);
    PREFETCH(0);

    {
      f32x2 w2f[7];
#pragma unroll
      for (int k = 0; k < 7; ++k) {
        u32 t;
        if (k < 4) t = ((const u32*)(smem + L_W2))[k * 256 + w * 64 + ln];
        else { asm volatile("" : "+v"(w2r[k])); t = w2r[k]; }
        w2f[k] = f32x2{bflo(t), bfhi(t)};
        asm volatile("" : "+v"(w2f[k]));
      }
      __builtin_amdgcn_s_sleep(DLY_C1);
      u32 hv[7];
_Pragma("unroll 1")
      for (int spin = 0; spin < SPIN_CAP; ++spin) {
        CLOBBER();
        u32 mxw = 0;
#pragma unroll
        for (int k = 0; k < 7; ++k) hv[k] = __builtin_amdgcn_raw_buffer_load_b32(rsW, ln * 4, OFF_H + layer * 8192 + w * 256 + k * 1024, AUX_SC1);
#pragma unroll
        for (int k = 0; k < 7; ++k) mxw = umax(mxw, hv[k]);
        if (__all(mxw != SENT)) break;
        __builtin_amdgcn_s_sleep(DLY_RETRY);
      }
      STAMP(10 + 12 * layer);
      f32x2 a = u2f(hv[0]) * w2f[0];
#pragma unroll
      for (int k = 1; k < 7; ++k) a += u2f(hv[k]) * w2f[k];
      float r = swap32_add(a.x, a.y);
      r = swap16_add(r, r);
      r += dppf<XOR1>(r); r += dppf<XOR2>(r); r += dppf<HMIR>(r); r += dppf<MIR>(r);
      float* const rd = red + 2 * RED_STRIDE;
      if ((ln & 31) == 0) rd[(ln >> 5) * 4 + w] = r;
      BAR();
      if (w == 0 && ln == 0) {
        const f32x2 x1 = *(const f32x2*)(red + 3 * RED_STRIDE);
        const float y0 = x1.x + sum4(*(const f32x4*)rd), y1 = x1.y + sum4(*(const f32x4*)(rd + 4));
        if (layer == 0) __builtin_amdgcn_raw_buffer_store_b64(v2u{canon(f2u(y0)), canon(f2u(y1))}, rsW, 0, OFF_X2 + dcol * 4, AUX_SC1);
        else *(u32*)(p.out + dcol) = pack_bf16(y0, y1);
      }
    }
    STAMP(11 + 12 * layer);
    if (layer == 1) break;
    PREFETCH(1);

    {
      v4u wt[4];
#pragma unroll
      for (int i = 0; i < 4; ++i) wt[i] = ldsWq[i * 256 + w * 64 + ln];
      f32x2 wf[2][8];
      unpack_w(wt, wf);
      float csq = sum4(*(const f32x4*)(red + 4 * RED_STRIDE + (ln & 15) * 4));
      asm volatile("" : "+v"(csq));
      __builtin_amdgcn_s_sleep(DLY_C2);
      v2u g;
_Pragma("unroll 1")
      for (int spin = 0; spin < SPIN_CAP; ++spin) {
        CLOBBER();
        g = __builtin_amdgcn_raw_buffer_load_b64(rsW, (ln & 31) * 16 + (ln >> 5) * 8, OFF_X2 + w * 512, AUX_SC1);
        if (__all(umax(g.x, g.y) != SENT)) break;
        __builtin_amdgcn_s_sleep(DLY_RETRY);
      }
      STAMP(12);
      const float xia = u2f(g.x), xib = u2f(g.y);
      ((v2u*)(smem + L_XI))[w * 64 + ln] = g;
      ln_gemv_part(xia - cmean, xib - cmean, wf, red, w, ln);
      BAR();
      float mu;
      const float qv = ln_gemv_final(red, csq, ln, mu);
      const u32 pk = canon(pack_bf16(qv, dppf<XOR1>(qv)));
      if (w == 0 && ln < 16 && (ln & 1) == 0) __builtin_amdgcn_raw_buffer_store_b32(pk, rsW, ln * 2, OFF_Q1 + qcol * 8, AUX_SC1);
      cmean = u2f((u32)__builtin_amdgcn_readfirstlane((int)f2u(cmean + mu)));
      STAMP(13);
    }
    int qo = voffK;
    asm volatile("" : "+v"(qo));
    qo = qoff(qo);
    __builtin_amdgcn_s_sleep(DLY_D);
_Pragma("unroll 1")
    for (int spin = 0; spin < SPIN_CAP; ++spin) {
      CLOBBER();
      qf = ld128<AUX_SC1>(rsW, qo, OFF_Q1 + kvh * 4096);
      if (__all(max4(qf) != SENT)) break;
    }
    PREFETCH(2);
  }
}

extern "C" void kernel_launch(void* const* d_in, const int* in_sizes, int n_in, void* d_out, int out_size,
                              void* d_ws, size_t ws_size, hipStream_t stream) {
  Params p;
  p.x = (const u16*)d_in[0];
  p.K = (const u16*)d_in[1];
  p.V = (const u16*)d_in[2];
  p.wq = (const u16*)d_in[3];
  p.wo = (const u16*)d_in[4];
  p.w1 = (const u16*)d_in[6];
  p.w3 = (const u16*)d_in[7];
  p.w2 = (const u16*)d_in[8];
  p.out = (u16*)d_out;
  p.ws = (unsigned char*)d_ws;
  decode_kernel<<<256, 256, 0, stream>>>(p);
}
```

```cpp
#include <hip/hip_runtime.h>
#include <stdint.h>

typedef unsigned u32;
typedef unsigned long long u64;
typedef unsigned short u16;
typedef u32 v4u __attribute__((ext_vector_type(4)));
typedef u32 v2u __attribute__((ext_vector_type(2)));
typedef float f32x4 __attribute__((ext_vector_type(4)));
typedef float f32x2 __attribute__((ext_vector_type(2)));
typedef __bf16 bf16x8 __attribute__((ext_vector_type(8)));
typedef __bf16 bf16x2 __attribute__((ext_vector_type(2)));
typedef __amdgpu_buffer_rsrc_t rsrc_t;

#define KV_AUX 2
#define W_AUX 0
#define RING 10

constexpr int AUX_SC1 = 16;
constexpr u32 SENT = 0xFFFFFFFFu;
constexpr int SPIN_CAP = 200000;

constexpr int OFF_EPOCH = 0;
constexpr int OFF_READY = 4096;
constexpr int OFF_Q0 = 8192;
constexpr int OFF_HO = OFF_Q0 + 32768;
constexpr int PA_REC = 1152;
constexpr int OFF_PA = OFF_HO;
constexpr int OFF_XB = OFF_PA + 2 * 16 * 32 * PA_REC;
constexpr int OFF_H = OFF_XB + 2 * 16 * 2048;
constexpr int OFF_X2 = OFF_H + 2 * 8192;
constexpr int OFF_FL = OFF_X2 + 2048;
constexpr int OFF_Q1 = OFF_FL + 2048;
constexpr int HO_USED = OFF_Q1 + 32768 - OFF_HO;
constexpr int HO_SLICE = 5120;
static_assert(HO_USED <= 256 * HO_SLICE, "reset slices must cover the hand-off region");
constexpr int OFF_STAMP = 4 << 20;
constexpr int WS_BYTES = OFF_HO + 256 * HO_SLICE;

constexpr int RED_STRIDE = 80;
constexpr int L_RED = 0;
constexpr int L_ML = L_RED + 6 * RED_STRIDE * 4;
constexpr int L_MRG = L_ML + 128;
constexpr int L_RX = L_MRG + 1024;
constexpr int L_XI = L_MRG + 8192;
constexpr int L_W2 = L_XI + 2048;
constexpr int L_WQ = L_W2 + 4096;
constexpr int L_W13 = L_WQ + 16384;
constexpr int L_V = L_W13 + 16384;
constexpr int L_BYTES = L_V + 114688;
static_assert(L_BYTES <= 163840 && L_V % 16 == 0, "LDS");

#define DLY_A 12
#define DLY_B 0
#define DLY_C1 12
#define DLY_C2 12
#define DLY_D 5
#define DLY_RETRY 3

struct Params {
  const u16* x; const u16* K; const u16* V; const u16* wq; const u16* wo;
  const u16* w1; const u16* w3; const u16* w2; u16* out; unsigned char* ws;
};

__device__ __forceinline__ float u2f(u32 x) { return __builtin_bit_cast(float, x); }
__device__ __forceinline__ u32 f2u(float x) { return __builtin_bit_cast(u32, x); }
__device__ __forceinline__ float bflo(u32 x) { return u2f(x << 16); }
__device__ __forceinline__ float bfhi(u32 x) { return u2f(x & 0xffff0000u); }
__device__ __forceinline__ u32 pack_bf16(float lo, float hi) {
  bf16x2 r = {(__bf16)lo, (__bf16)hi};
  return __builtin_bit_cast(u32, r);
}
__device__ __forceinline__ u32 canon(u32 x) { return x == SENT ? 0x7FC07FC0u : x; }
template <int CTRL> __device__ __forceinline__ float dppf(float x) {
  return u2f((u32)__builtin_amdgcn_mov_dpp((int)f2u(x), CTRL, 0xf, 0xf, true));
}
template <int CTRL> __device__ __forceinline__ u32 dppu(u32 x) {
  return (u32)__builtin_amdgcn_mov_dpp((int)x, CTRL, 0xf, 0xf, true);
}
constexpr int XOR1 = 0xB1, XOR2 = 0x4E, XOR3 = 0x1B, HMIR = 0x141, MIR = 0x140, ROR8 = 0x128;
__device__ __forceinline__ float swap32_add(float a, float b) {
  auto r = __builtin_amdgcn_permlane32_swap(f2u(a), f2u(b), false, false);
  return u2f(r[0]) + u2f(r[1]);
}
__device__ __forceinline__ float swap16_add(float a, float b) {
  auto r = __builtin_amdgcn_permlane16_swap(f2u(a), f2u(b), false, false);
  return u2f(r[0]) + u2f(r[1]);
}
__device__ __forceinline__ float xrow_max(float x) {
  auto s = __builtin_amdgcn_permlane16_swap(f2u(x), f2u(x), false, false);
  x = fmaxf(u2f(s[0]), u2f(s[1]));
  auto t = __builtin_amdgcn_permlane32_swap(f2u(x), f2u(x), false, false);
  return fmaxf(u2f(t[0]), u2f(t[1]));
}
__device__ __forceinline__ float xrow_sum(float x) {
  x = swap16_add(x, x);
  return swap32_add(x, x);
}
__device__ __forceinline__ float wave_sum(float x) {
  x += dppf<XOR1>(x); x += dppf<XOR2>(x); x += dppf<HMIR>(x); x += dppf<MIR>(x);
  return xrow_sum(x);
}
__device__ __forceinline__ float treduce16(float (&v)[16], int lane) {
#pragma unroll
  for (int i = 0; i < 8; ++i) v[i] = swap32_add(v[i], v[i + 8]);
#pragma unroll
  for (int i = 0; i < 4; ++i) v[i] = swap16_add(v[i], v[i + 4]);
  const bool b3 = lane & 8, b2 = lane & 4;
#pragma unroll
  for (int i = 0; i < 2; ++i) {
    const float keep = b3 ? v[i + 2] : v[i], send = b3 ? v[i] : v[i + 2];
    v[i] = keep + dppf<ROR8>(send);
  }
  const float keep = b2 ? v[1] : v[0], send = b2 ? v[0] : v[1];
  float r = keep + dppf<HMIR>(send);
  r += dppf<XOR2>(r);
  r += dppf<XOR1>(r);
  return r;
}
__device__ __forceinline__ float treduce8(float (&v)[8], int lane) {
#pragma unroll
  for (int i = 0; i < 4; ++i) v[i] = swap32_add(v[i], v[i + 4]);
#pragma unroll
  for (int i = 0; i < 2; ++i) v[i] = swap16_add(v[i], v[i + 2]);
  const bool b3 = lane & 8;
  const float keep = b3 ? v[1] : v[0], send = b3 ? v[0] : v[1];
  float r = keep + dppf<ROR8>(send);
  r += dppf<XOR1>(r);
  r += dppf<XOR2>(r);
  r += dppf<HMIR>(r);
  return r;
}
__device__ __forceinline__ f32x4 mfma(v4u a, v4u b, f32x4 c) {
  return __builtin_amdgcn_mfma_f32_16x16x32_bf16(__builtin_bit_cast(bf16x8, a), __builtin_bit_cast(bf16x8, b), c, 0, 0, 0);
}
__device__ __forceinline__ f32x4 qk1(v4u k, v4u q) {
  f32x4 d;
  asm volatile("v_mfma_f32_16x16x32_bf16 %0, %1, %2, 0" : "=&v"(d) : "v"(k), "v"(q));
  return d;
}
__device__ __forceinline__ void qk_fence(f32x4 (&D)[4]) {
  asm volatile("s_nop 7" : "+v"(D[0]), "+v"(D[1]), "+v"(D[2]), "+v"(D[3]));
}
__device__ __forceinline__ void pv2a(f32x4& a0, f32x4& a1, v4u va, v4u vb, v4u pv) {
  asm volatile("s_nop 1\n\t"
      "v_mfma_f32_16x16x32_bf16 %0, %2, %4, %0\n\t"
      "v_mfma_f32_16x16x32_bf16 %1, %3, %4, %1"
      : "+v"(a0), "+v"(a1) : "a"(va), "a"(vb), "v"(pv));
}
__device__ __forceinline__ void pv2v(f32x4& a0, f32x4& a1, v4u va, v4u vb, v4u pv) {
  asm volatile("s_nop 1\n\t"
      "v_mfma_f32_16x16x32_bf16 %0, %2, %4, %0\n\t"
      "v_mfma_f32_16x16x32_bf16 %1, %3, %4, %1"
      : "+v"(a0), "+v"(a1) : "v"(va), "v"(vb), "v"(pv));
}
__device__ __forceinline__ v4u ldw(const void* base, u32 off) {
  const v4u* q = (const v4u*)((const unsigned char*)base + off);
  return *q;
}
constexpr int home(int T, int es) { return T >= 12 ? 2 : T == 11 ? (es < 2 ? 1 : 2) : (T % 3 == 2 ? 1 : 0); }
constexpr int hidx(int T, int es) { return T >= 12 ? T - 12 : T == 11 ? (es < 2 ? 3 : 4) : (T % 3 == 2 ? T / 3 : T - T / 3); }
constexpr int lslot(int T, int e) { return T == 11 ? 24 + (e >> 2) * 2 + (e & 3) : (T / 3) * 8 + e; }
template <int AUX> __device__ __forceinline__ v4u ld128(rsrc_t rs, int voff, int soff) {
  return __builtin_amdgcn_raw_buffer_load_b128(rs, voff, soff, AUX);
}
#define KEEPALL 0
#define ROTMUL 5
#define ROTMUL2 16
#define L2MOD 5
#define L2MASK 4
__device__ __forceinline__ int chunk(int j, int rot4k) { return (j * 4096 + rot4k) & 0x7FFFF; }
constexpr int NPRE = 8;
#define UNTOUCH 0
#define PROMOTE 0
#define MALLM 2
#define MALLN 1
#define CODEPF 3
#define KEEP0 104
constexpr bool kept(int j) { return j >= KEEP0; }
constexpr int order2(int st) { return st == 0 ? 12 : st < 7 ? ((st & 1) ? st / 2 : 12 + st / 2) : st - 4; }
constexpr int blk2(int i) { return 8 * order2(i >> 3) + (i & 7); }
constexpr int hbm_before(int st) { int n = 0; for (int i = NPRE; i < 8 * st; ++i) n += kept(blk2(i)) ? 0 : 1; return n; }
template <bool FIRST> __device__ __forceinline__ v4u ldk(rsrc_t rs, int voff, int j, int rot4k) {
  if (FIRST && (kept(j) || (j < 96 && j % MALLM < MALLN))) return ld128<0>(rs, voff, chunk(j, rot4k));
  return ld128<KV_AUX>(rs, voff, chunk(j, rot4k));
}
#define W1 18
#define W2A 10
#define W2B 18
#define W2C 20
#define W2D 12
constexpr int win2(int i) { return i < 8 ? W2A : i < 24 ? W2B : i < 48 ? W2C : W2D; }
constexpr int cmin(int a, int b) { return a < b ? a : b; }
constexpr int cmax(int a, int b) { return a > b ? a : b; }
constexpr int hi_start(int st);
constexpr int hi_after(int i) { return i < 0 ? NPRE : cmax((i & 7) ? hi_after(i - 1) : hi_start(i >> 3), cmin(128, i + 1 + win2(i))); }
constexpr int hi_start(int st) { return cmax(hi_after(8 * st - 1), cmin(128, 8 * st + win2(8 * st))); }
__device__ __forceinline__ int qoff(int voffK) { return ((voffK >> 5) & 31) * 128 + (voffK & 31); }
__device__ __forceinline__ u32 umax(u32 a, u32 b) { return a > b ? a : b; }
__device__ __forceinline__ u32 max4(v4u v) { return umax(umax(v.x, v.y), umax(v.z, v.w)); }
#define BAR() asm volatile("s_waitcnt lgkmcnt(0)\n\ts_barrier" ::: "memory")
#define CLOBBER() asm volatile("" ::: "memory")
#define SCHED() __builtin_amdgcn_sched_barrier(0)
#define TILEBAR 1
#define PACE_OFF1 300
#define PACE_OFF2 2000
#define PACE_OFF3 0
#define PACE2 840
#define PACE1 0
#define PACE 920
__device__ __forceinline__ void pace(u32 t0, int due) {
  if (PACE) {
    asm volatile("1:\n\ts_memrealtime vcc\n\ts_waitcnt lgkmcnt(0)\n\ts_sub_u32 vcc_lo, vcc_lo, %0\n\t"
                 "s_cmp_lt_i32 vcc_lo, %1\n\ts_cbranch_scc1 1b" ::"s"(t0), "s"(due) : "vcc", "scc", "memory");
  }
  if (TILEBAR) __builtin_amdgcn_s_barrier();
}

#define STAMP(i) do { } while (0)

__device__ __forceinline__ void fma8(float* acc, float x, v4u w) {
  acc[0] = fmaf(x, bflo(w.x), acc[0]); acc[1] = fmaf(x, bfhi(w.x), acc[1]);
  acc[2] = fmaf(x, bflo(w.y), acc[2]); acc[3] = fmaf(x, bfhi(w.y), acc[3]);
  acc[4] = fmaf(x, bflo(w.z), acc[4]); acc[5] = fmaf(x, bfhi(w.z), acc[5]);
  acc[6] = fmaf(x, bflo(w.w), acc[6]); acc[7] = fmaf(x, bfhi(w.w), acc[7]);
}

__device__ __forceinline__ void unpack_w(const v4u (&wt)[4], f32x2 (&wf)[2][8]) {
#pragma unroll
  for (int r = 0; r < 2; ++r)
#pragma unroll
    for (int i = 0; i < 4; ++i) {
      wf[r][i] = f32x2{bflo(wt[r][i]), bfhi(wt[r][i])};
      wf[r][4 + i] = f32x2{bflo(wt[2 + r][i]), bfhi(wt[2 + r][i])};
    }
#pragma unroll
  for (int r = 0; r < 2; ++r)
#pragma unroll
    for (int i = 0; i < 8; ++i) asm volatile("" : "+v"(wf[r][i]));
}
__device__ __forceinline__ void ln_gemv_part(float da, float db, const f32x2 (&wf)[2][8], float* red, int w, int lane) {
  float a[16];
#pragma unroll
  for (int i = 0; i < 8; ++i) {
    const f32x2 t = da * wf[0][i] + db * wf[1][i];
    a[2 * i] = t.x; a[2 * i + 1] = t.y;
  }
  float st = swap32_add(da + db, fmaf(da, da, db * db));
  st = swap16_add(st, st);
  st += dppf<XOR1>(st); st += dppf<XOR2>(st); st += dppf<HMIR>(st); st += dppf<MIR>(st);
  const float r = treduce16(a, lane);
  const bool isr = (lane & 3) == 0;
  const int slot = isr ? (lane >> 2) : 16 + (lane >> 5);
  if (isr || (lane & 31) == 1) red[slot * 4 + w] = isr ? r : st;
}
__device__ __forceinline__ float sum4(f32x4 v) { return (v.x + v.y) + (v.z + v.w); }
__device__ __forceinline__ float ln_gemv_final(const float* red, float cs, int lane, float& mu) {
  const f32x4 g = *(const f32x4*)(red + (lane & 15) * 4), s1 = *(const f32x4*)(red + 64), s2 = *(const f32x4*)(red + 68);
  mu = sum4(s1) * (1.0f / 512.0f);
  const float var = fmaf(-mu, mu, sum4(s2) * (1.0f / 512.0f));
  return fmaf(-mu, cs, sum4(g)) * __builtin_amdgcn_rsqf(fmaxf(var, 0.f) + 1e-5f);
}

template <int I> struct IC { static constexpr int value = I; };
template <int I, int N, class F> __device__ __forceinline__ void static_for(F&& f) {
  if constexpr (I < N) { f(IC<I>{}); static_for<I + 1, N>(f); }
}

__global__ void __launch_bounds__(256, 1) decode_kernel(Params p) {
  __shared__ __attribute__((aligned(16))) unsigned char smem[L_BYTES];
  const int tid = threadIdx.x, lane = tid & 63, w = __builtin_amdgcn_readfirstlane(tid >> 6);
  const int b = blockIdx.x, kvh = b & 7, sp = b >> 3;
  u32 tp = PACE ? (u32)__builtin_amdgcn_s_memrealtime() : 0u;
  const rsrc_t rsW = __builtin_amdgcn_make_buffer_rsrc((void*)p.ws, 0, WS_BYTES, 0x00020000);
  const size_t kvoff = ((size_t)kvh * 65536 + (size_t)sp * 2048) * 128;
  const rsrc_t rsK = __builtin_amdgcn_make_buffer_rsrc((void*)(p.K + kvoff), 0, 2048 * 256, 0x00020000);
  const rsrc_t rsV = __builtin_amdgcn_make_buffer_rsrc((void*)(p.V + kvoff), 0, 2048 * 256, 0x00020000);

  float* const red = (float*)(smem + L_RED);
  u64 code0;
  u32 code_len;
  asm volatile("s_getpc_b64 %0\n.Ldk_code_begin:\n\ts_mov_b32 %1, .Ldk_code_end-.Ldk_code_begin" : "=s"(code0), "=s"(code_len));
  const rsrc_t rsC = __builtin_amdgcn_make_buffer_rsrc((void*)code0, 0, code_len, 0x00020000);

  const int rot4k = ((sp * ROTMUL + kvh * ROTMUL2) & 127) * 4096;
  int rotK1 = rot4k, rotK2 = rot4k;
  asm volatile("" : "+s"(rotK1));
  asm volatile("" : "+s"(rotK2));
  const int voffV = lane * 16 + w * 1024;
  const int voffK = ((lane >> 2) & 3) * 256 + ((lane >> 4) * 4 + (lane & 3)) * 16 + w * 1024;
  v4u vresA[8][8], vresV[5][8];
  v4u raw[3][8], rawL[2][8];
  v4u k1[128], k2[128];
#pragma unroll
  for (int T = 0; T < 3; ++T)
#pragma unroll
    for (int t = 0; t < 8; ++t) raw[T][t] = ld128<KV_AUX>(rsV, voffV, chunk(8 * T + t, rot4k));
  SCHED();
  u32 touch[6];
  touch[0] = *(const u32*)((const unsigned char*)p.wq + (u32)((16 * sp + 4 * w + (lane >> 4)) * 8192 + kvh * 1024 + (lane & 15) * 64));
  touch[3] = CODEPF ? __builtin_amdgcn_raw_buffer_load_b32(rsC, tid * 64, sp * 16384, 0) : 0u;
  if (w == 0) {
    const v4u s4 = {SENT, SENT, SENT, SENT};
#pragma unroll
    for (int i = 0; i < 5; ++i) __builtin_amdgcn_raw_buffer_store_b128(s4, rsW, OFF_HO + lane * 16 + i * 1024, b * HO_SLICE, AUX_SC1);
  }
  const v2u ep = __builtin_amdgcn_raw_buffer_load_b64(rsW, OFF_EPOCH, 0, AUX_SC1);
  const int row0 = w * 128 + 4 * (lane & 31) + 2 * (lane >> 5);
  const int qcol = kvh * 512 + sp * 16;
  const int rg = 2 * kvh + (sp & 1), cs = sp >> 1;
  const bool doS3 = sp < 28;
  const int s3 = kvh * 28 + (doS3 ? sp : 0);
  const int dcol = kvh * 64 + sp * 2;
  u32 x01 = 0, x00 = 0;
  SCHED();

  float cmean = 0.f;
  u32 tag_lo = 0, tag_hi = 0;
  u32 w2r[7];
  v4u qf = {0u, 0u, 0u, 0u}, r0 = qf, r1 = qf;
  v4u wtmp[8], wot[4];
  v4u* const ldsV = (v4u*)(smem + L_V) + w * (28 * 64) + lane;
  v4u* const ldsWq = (v4u*)(smem + L_WQ);
  v4u* const ldsW13 = (v4u*)(smem + L_W13);
  auto ingest = [&](auto TC, v4u (&rw)[8], auto&& between) __attribute__((always_inline)) {
    constexpr int T = decltype(TC)::value;
    v4u f[8];
#pragma unroll
    for (int e = 0; e < 8; ++e)
#pragma unroll
      for (int j = 0; j < 4; ++j)
        f[e][j] = __builtin_amdgcn_perm(rw[j + 4][e >> 1], rw[j][e >> 1], (e & 1) ? 0x07060302u : 0x05040100u);
#pragma unroll
    for (int e = 0; e < 8; ++e)
      if (home(T, e & 3) == 2) asm volatile("" : "+v"(f[e]));
    SCHED();
    between();
#pragma unroll
    for (int e = 0; e < 8; ++e) {
      if (home(T, e & 3) == 1) ldsV[lslot(T, e) * 64] = f[e];
      else if (home(T, e & 3) == 2) vresV[hidx(T, e & 3)][e] = f[e];
      else { asm volatile("" : "+a"(f[e])); vresA[hidx(T, e & 3)][e] = f[e]; }
    }
    SCHED();
  };
  auto issue1 = [&](auto UC) __attribute__((always_inline)) {
    constexpr int u = decltype(UC)::value;
    if constexpr (u < 112) k1[u] = ldk<true>(rsK, voffK, u, rotK1);
    else if constexpr (u < 120) rawL[0][u - 112] = ld128<KV_AUX>(rsV, voffV, chunk(u, rot4k));
    else if constexpr (u < 128) k1[u - 8] = ldk<true>(rsK, voffK, u - 8, rotK1);
    else if constexpr (u < 136) rawL[1][u - 128] = ld128<KV_AUX>(rsV, voffV, chunk(u - 8, rot4k));
    else if constexpr (u < 144) k1[u - 16] = ldk<true>(rsK, voffK, u - 16, rotK1);
  };
  static_for<0, 14>([&](auto TC) __attribute__((always_inline)) {
    constexpr int T = decltype(TC)::value;
    ingest(TC, raw[T % 3], [&]() __attribute__((always_inline)) {
    pace(tp, T * PACE1 / 8 + PACE_OFF1);
    SCHED();
    if constexpr (T + 3 < 14) {
#pragma unroll
      for (int t = 0; t < 8; ++t) raw[T % 3][t] = ld128<KV_AUX>(rsV, voffV, chunk(8 * (T + 3) + t, rot4k));
    } else {
      static_for<(T - 11) * W1 / 3, (T - 10) * W1 / 3>(issue1);
    }
    if constexpr (T == 0) {
      const int l7 = lane < 56 ? lane : 55;
      touch[1] = *(const u32*)((const unsigned char*)(w < 2 ? p.w1 : p.w3) + (u32)((16 * sp + 8 * (w & 1) + l7 / 7) * 3584 + kvh * 448 + (l7 % 7) * 64));
    }
    if constexpr (T == 1) {
      x01 = *(const u32*)(p.x + row0);
      x00 = *(const u32*)p.x;
      const int l2 = lane < 28 ? lane : 27;
      touch[2] = *(const u32*)((const unsigned char*)p.w2 + (u32)((56 * sp + 14 * w + (l2 >> 1)) * 1024 + kvh * 128 + (l2 & 1) * 64));
    }
    if constexpr (T == 1 || T == 2) {
#pragma unroll
      for (int i = 2 * (T - 1); i < 2 * (T - 1) + 2; ++i) wtmp[i] = ldw(p.wq, (w * 128 + 32 * i + (lane >> 1)) * 8192 + qcol * 2 + (lane & 1) * 16);
    }
    if constexpr (T >= 3 && T < 7) wtmp[T + 1] = ldw(T < 5 ? p.w1 : p.w3, (row0 + ((T + 1) & 1)) * 3584 + s3 * 16);
    if constexpr (T >= 5 && T < 12) w2r[T - 5] = *(const u32*)((const unsigned char*)p.w2 + (u32)((tid + 256 * (T - 5)) * 1024 + dcol * 2));
    if constexpr (T >= 7 && T < 11) {
      const int pp = 16 * w + (lane >> 2);
      const int orow = rg * 256 + ((pp >> 2) & 1) * 128 + ((pp >> 3) & 3) * 32 + (pp >> 5) * 4 + (pp & 3);
      wot[T - 7] = ldw(p.wo, (orow + 8 * (T - 7)) * 1024 + cs * 64 + (lane & 3) * 16);
    }
    if constexpr (T == 5) touch[4] = __builtin_amdgcn_raw_buffer_load_b32(rsK, lane * 8192, 0, KV_AUX);
    if constexpr (T == 9) touch[5] = *(const u32*)(p.out + 2 * lane);
    if constexpr (T >= 8 && T < 12) ((u32*)(smem + L_W2))[(T - 8) * 256 + tid] = w2r[T - 8];
    if constexpr (T == 5) asm volatile("" ::"v"(touch[0]), "v"(touch[1]), "v"(touch[2]), "v"(touch[3]));
    if constexpr (T == 13) asm volatile("" ::"v"(touch[4]), "v"(touch[5]));
    });
    if constexpr (T == 0) STAMP(24);
    if constexpr (T == 5) {
#pragma unroll
      for (int i = 0; i < 4; ++i) {
        const int rho = 32 * i + (lane >> 1);
        ldsWq[((lane & 1) * 2 + (rho & 1)) * 256 + w * 64 + (rho >> 2) + 32 * ((rho >> 1) & 1)] = wtmp[i];
      }
      v4u wt[4];
#pragma unroll
      for (int i = 0; i < 4; ++i) wt[i] = ldsWq[i * 256 + tid];
      f32x2 wf[2][8];
      unpack_w(wt, wf);
      const float xia = bflo(x01), xib = bfhi(x01);
      ((f32x2*)(smem + L_XI))[tid] = f32x2{xia, xib};
      cmean = bflo(x00);
      ln_gemv_part(1.0f, 1.0f, wf, red + 4 * RED_STRIDE, w, lane);
      ln_gemv_part(xia - cmean, xib - cmean, wf, red, w, lane);
      BAR();
      const float csq = sum4(*(const f32x4*)(red + 4 * RED_STRIDE + (lane & 15) * 4));
      float mu;
      const float qv = ln_gemv_final(red, csq, lane, mu);
      cmean = u2f((u32)__builtin_amdgcn_readfirstlane((int)f2u(cmean + mu)));
      const u32 pk = canon(pack_bf16(qv, dppf<XOR1>(qv)));
      if (w == 0 && lane < 16 && (lane & 1) == 0) __builtin_amdgcn_raw_buffer_store_b32(pk, rsW, lane * 2, OFF_Q0 + qcol * 8, AUX_SC1);
      const u64 epoch = (((u64)ep.y << 32) | ep.x) + 0x9E3779B97F4A7C15ull;
      tag_lo = (u32)__builtin_amdgcn_readfirstlane((int)(u32)epoch);
      tag_hi = (u32)__builtin_amdgcn_readfirstlane((int)(u32)(epoch >> 32));
      STAMP(1);
      SCHED();
    }
    if constexpr (T == 7) if (w == 0) {
      asm volatile("s_waitcnt vmcnt(16)" ::: "memory");
      if (lane == 0) __builtin_amdgcn_raw_buffer_store_b64(v2u{tag_lo, tag_hi}, rsW, 0, OFF_READY + b * 8, AUX_SC1);
      SCHED();
    }
    if constexpr (T == 9) {
      const v4u wt[4] = {wtmp[4], wtmp[5], wtmp[6], wtmp[7]};
#pragma unroll
      for (int i = 0; i < 4; ++i) ldsW13[i * 256 + tid] = wt[i];
      f32x2 wf[2][8];
      unpack_w(wt, wf);
      ln_gemv_part(1.0f, 1.0f, wf, red + 5 * RED_STRIDE, w, lane);
      SCHED();
    }
    if constexpr (T == 11) {
      r0 = ld128<AUX_SC1>(rsW, OFF_READY + lane * 16, 0);
      r1 = ld128<AUX_SC1>(rsW, OFF_READY + 1024 + lane * 16, 0);
      SCHED();
    }
    if constexpr (T == 13) {
      auto not_ready = [&](v4u a, v4u c) {
        const u32 bad = ((a.x ^ tag_lo) | (a.y ^ tag_hi) | (a.z ^ tag_lo) | (a.w ^ tag_hi)) |
                        ((c.x ^ tag_lo) | (c.y ^ tag_hi) | (c.z ^ tag_lo) | (c.w ^ tag_hi));
        return __any(bad != 0);
      };
      if (__builtin_expect(not_ready(r0, r1), 0)) {
_Pragma("unroll 1")
        for (int spin = 0; spin < SPIN_CAP; ++spin) {
          __builtin_amdgcn_s_sleep(8);
          CLOBBER();
          const v4u a = ld128<AUX_SC1>(rsW, OFF_READY + lane * 16, 0), c = ld128<AUX_SC1>(rsW, OFF_READY + 1024 + lane * 16, 0);
          if (!not_ready(a, c)) break;
        }
      }
      CLOBBER();
      qf = ld128<AUX_SC1>(rsW, qoff(voffK), OFF_Q0 + kvh * 4096);
      if (b == 0 && tid == 0) __builtin_amdgcn_raw_buffer_store_b64(v2u{tag_lo, tag_hi}, rsW, OFF_EPOCH, 0, AUX_SC1);
      SCHED();
    }
  });
  STAMP(2);

  const int c4 = lane & 3;
  const bool cb0 = lane & 1, cb1 = lane & 2;
  constexpr float C2 = 0.08838834764831845f * 1.4426950408889634f;

  auto issue2 = [&](auto IC_) __attribute__((always_inline)) {
    constexpr int i = decltype(IC_)::value;
    k2[blk2(i)] = ldk<false>(rsK, voffK, blk2(i), rotK2);
  };
  auto layer_body = [&](auto LC) __attribute__((always_inline)) {
    constexpr int layer = decltype(LC)::value;
    STAMP(3 + 12 * layer);
    if (PACE && layer) tp = (u32)__builtin_amdgcn_s_memrealtime();
    constexpr int tbase = layer ? PACE_OFF3 : PACE_OFF2, tstep = layer ? PACE2 / 8 : PACE / 8;
    float m = -INFINITY, mc = 0.f, l = 0.f;
    f32x4 acc[2] = {f32x4{0.f, 0.f, 0.f, 0.f}, f32x4{0.f, 0.f, 0.f, 0.f}};
    u32 junk[4] = {0u, 0u, 0u, 0u};
    int ln = lane;
    asm volatile("s_nop 1" : "+v"(qf));
    static_for<0, 16>([&](auto SC) __attribute__((always_inline)) {
      constexpr int st = decltype(SC)::value;
      constexpr int T = layer ? order2(st) : st;
      if constexpr (st > 0 && st < 15) { constexpr int due = layer ? hbm_before(st) * PACE2 / 64 + tbase : st * tstep + tbase; pace(tp, due); SCHED(); }
      if constexpr (layer == 1) { static_for<hi_after(8 * st - 1), hi_start(st)>(issue2); SCHED(); }
      if constexpr (layer == 0 && T >= 14)
        ingest(IC<T>{}, rawL[T - 14], [&]() __attribute__((always_inline)) { static_for<112 + 16 * (T - 14) + W1, 120 + 16 * (T - 14) + W1>(issue1); });
      if constexpr (layer == 0 && UNTOUCH && st == 2) {
        const int l7 = lane < 56 ? lane : 55, l2 = lane < 28 ? lane : 27;
        junk[0] = __builtin_nontemporal_load((const u32*)((const unsigned char*)p.wq + (u32)((16 * sp + 4 * w + (lane >> 4)) * 8192 + kvh * 1024 + (lane & 15) * 64)));
        junk[1] = __builtin_nontemporal_load((const u32*)((const unsigned char*)(w < 2 ? p.w1 : p.w3) + (u32)((16 * sp + 8 * (w & 1) + l7 / 7) * 3584 + kvh * 448 + (l7 % 7) * 64)));
        junk[2] = __builtin_nontemporal_load((const u32*)((const unsigned char*)p.w2 + (u32)((56 * sp + 14 * w + (l2 >> 1)) * 1024 + kvh * 128 + (l2 & 1) * 64)));
        SCHED();
      }
      if constexpr (layer == 0 && UNTOUCH && st == 5) { asm volatile("" ::"v"(junk[0]), "v"(junk[1]), "v"(junk[2])); SCHED(); }
      if constexpr (layer == 0 && (CODEPF & 2) && st == 13) { junk[3] = __builtin_amdgcn_raw_buffer_load_b32(rsC, tid * 64, sp * 16384, AUX_SC1); SCHED(); }
      if constexpr (layer == 0 && (CODEPF & 2) && st == 15) { asm volatile("" ::"v"(junk[3])); SCHED(); }
      float X[2];
      static_for<0, 2>([&](auto HC) __attribute__((always_inline)) {
        constexpr int hf = decltype(HC)::value;
        f32x4 D[4];
        static_for<0, 4>([&](auto QC) __attribute__((always_inline)) {
          constexpr int t = decltype(QC)::value, blk = 8 * T + 4 * hf + t, i = 8 * st + 4 * hf + t;
          D[t] = qk1(layer ? k2[blk] : k1[blk], qf);
          if constexpr (layer == 0) issue1(IC<(blk < 112 ? blk : blk < 120 ? blk + 8 : blk + 16) + W1>{});
          else static_for<((i & 7) ? hi_after(i - 1) : hi_start(st)), hi_after(i)>(issue2);
          SCHED();
        });
        qk_fence(D);
        SCHED();
        const float e0 = cb1 ? (cb0 ? D[3][3] : D[2][2]) : (cb0 ? D[1][1] : D[0][0]);
        const float e1 = cb1 ? (cb0 ? D[2][3] : D[3][2]) : (cb0 ? D[0][1] : D[1][0]);
        const float e2 = cb1 ? (cb0 ? D[1][3] : D[0][2]) : (cb0 ? D[3][1] : D[2][0]);
        const float e3 = cb1 ? (cb0 ? D[0][3] : D[1][2]) : (cb0 ? D[2][1] : D[3][0]);
        X[hf] = (e0 + dppf<XOR1>(e1)) + (dppf<XOR2>(e2) + dppf<XOR3>(e3));
      });
      if constexpr (layer == 0 && PROMOTE && kept(8 * T)) {
        junk[T & 3] = __builtin_amdgcn_raw_buffer_load_b32(rsK, ((8 * T + (lane >> 3)) * 4096 + rotK1 & 0x7FFFF) + w * 1024 + (lane & 7) * 128, 0, AUX_SC1);
        SCHED();
      }
      float mx = fmaxf(X[0], X[1]);
      if (__any(mx > m)) {
        mx = fmaxf(mx, dppf<XOR1>(mx));
        mx = fmaxf(mx, dppf<XOR2>(mx));
        mx = xrow_max(mx);
        const float mn = fmaxf(m, mx);
        const float mcn = (mn == -INFINITY) ? 0.f : mn * C2;
        const float alpha = __builtin_amdgcn_exp2f(m * C2 - mcn);
        l *= alpha;
        acc[0] *= alpha; acc[1] *= alpha;
        m = mn; mc = mcn;
      }
      const float p0 = __builtin_amdgcn_exp2f(fmaf(X[0], C2, -mc)), p1 = __builtin_amdgcn_exp2f(fmaf(X[1], C2, -mc));
      l += p0 + p1;
      const u32 Y = pack_bf16(p0, p1);
      const v4u Yb = {dppu<0x00>(Y), dppu<0x55>(Y), dppu<0xAA>(Y), dppu<0xFF>(Y)};
#pragma unroll
      for (int es = 0; es < 4; ++es) {
        const bool on = c4 == es;
        const v4u pv = {on ? Yb.x : 0u, on ? Yb.y : 0u, on ? Yb.z : 0u, on ? Yb.w : 0u};
        if (home(T, es) == 0) pv2a(acc[0], acc[1], vresA[hidx(T, es)][es], vresA[hidx(T, es)][4 + es], pv);
        else if (home(T, es) == 2) pv2v(acc[0], acc[1], vresV[hidx(T, es)][es], vresV[hidx(T, es)][4 + es], pv);
        else pv2v(acc[0], acc[1], ldsV[lslot(T, es) * 64], ldsV[lslot(T, 4 + es) * 64], pv);
      }
      SCHED();
    });
    STAMP(4 + 12 * layer);
    asm volatile("" : "+v"(ln));

    l += dppf<XOR1>(l); l += dppf<XOR2>(l); l = xrow_sum(l);
    asm volatile("s_nop 7" : "+v"(acc[0]), "+v"(acc[1]));
    {
      f32x4* mrg = (f32x4*)(smem + L_MRG);
      mrg[(w * 2 + 0) * 64 + ln] = acc[0];
      mrg[(w * 2 + 1) * 64 + ln] = acc[1];
      f32x2* ml = (f32x2*)(smem + L_ML);
      if (ln < 16 && (ln & 3) == 0) ml[(ln >> 2) * 4 + w] = f32x2{mc, l};
      BAR();
      if (w < 2) {
        const int h = (ln >> 2) & 3;
        const f32x4 qa = *(const f32x4*)(ml + h * 4), qb = *(const f32x4*)(ml + h * 4 + 2);
        const float M = fmaxf(fmaxf(qa.x, qa.z), fmaxf(qb.x, qb.z));
        const float f0 = __builtin_amdgcn_exp2f(qa.x - M), f1 = __builtin_amdgcn_exp2f(qa.z - M);
        const float f2 = __builtin_amdgcn_exp2f(qb.x - M), f3 = __builtin_amdgcn_exp2f(qb.z - M);
        const f32x4 o = (f0 * mrg[w * 64 + ln] + f1 * mrg[(2 + w) * 64 + ln]) + (f2 * mrg[(4 + w) * 64 + ln] + f3 * mrg[(6 + w) * 64 + ln]);
        const float L = fmaf(f0, qa.y, f1 * qa.w) + fmaf(f2, qb.y, f3 * qb.w);
        const int pos = (w * 32 + (ln >> 4) * 8 + (h & 1) * 4 + (ln & 3)) * 16 + (h >> 1) * (32 * PA_REC);
        const int rec = OFF_PA + ((layer * 16 + 2 * kvh) * 32 + sp) * PA_REC;
        const v4u ov = {canon(f2u(o.x)), canon(f2u(o.y)), canon(f2u(o.z)), canon(f2u(o.w))};
        __builtin_amdgcn_raw_buffer_store_b128(ov, rsW, pos, rec, AUX_SC1);
        if (w == 0 && ln < 16 && (ln & 3) == 0)
          __builtin_amdgcn_raw_buffer_store_b64(v2u{canon(f2u(M)), canon(f2u(L))}, rsW, 1024 + (h & 1) * 8 + (h >> 1) * (32 * PA_REC), rec, AUX_SC1);
      }
    }
    STAMP(5 + 12 * layer);

    {
      if (w >= 2) __builtin_amdgcn_s_sleep(8);
      if (DLY_A) __builtin_amdgcn_s_sleep(DLY_A);
      v4u pa[8];
      v2u mlv;
      const int base = OFF_PA + (layer * 16 + rg) * 32 * PA_REC;
      CLOBBER();
      mlv = __builtin_amdgcn_raw_buffer_load_b64(rsW, 1024 + (ln >> 1) * PA_REC + (ln & 1) * 8, base, AUX_SC1);
#pragma unroll
      for (int k = 0; k < 8; ++k) pa[k] = ld128<AUX_SC1>(rsW, ln * 16, base + (8 * w + k) * PA_REC);
      SCHED();
      if constexpr (layer == 0 && PROMOTE) asm volatile("" ::"v"(junk[0]), "v"(junk[1]), "v"(junk[2]), "v"(junk[3]));
      f32x2 wof[4][4];
#pragma unroll
      for (int r = 0; r < 4; ++r)
#pragma unroll
        for (int i = 0; i < 4; ++i) { if (i == 0) asm volatile("" : "+v"(wot[r])); wof[r][i] = f32x2{bflo(wot[r][i]), bfhi(wot[r][i])}; asm volatile("" : "+v"(wof[r][i])); }
      SCHED();
_Pragma("unroll 1")
      for (int spin = 0; spin < SPIN_CAP; ++spin) {
        u32 mk[8], mxw = umax(mlv.x, mlv.y);
#pragma unroll
        for (int k = 0; k < 8; ++k) { mk[k] = max4(pa[k]); mxw = umax(mxw, mk[k]); }
        if (__all(mxw != SENT)) break;
        u32 need = __any(umax(mlv.x, mlv.y) == SENT) ? 0x100u : 0u;
#pragma unroll
        for (int k = 0; k < 8; ++k) need |= __any(mk[k] == SENT) ? (1u << k) : 0u;
        CLOBBER();
        if (need & 0x100) mlv = __builtin_amdgcn_raw_buffer_load_b64(rsW, 1024 + (ln >> 1) * PA_REC + (ln & 1) * 8, base, AUX_SC1);
#pragma unroll
        for (int k = 0; k < 8; ++k)
          if (need & (1 << k)) pa[k] = ld128<AUX_SC1>(rsW, ln * 16, base + (8 * w + k) * PA_REC);
        SCHED();
      }
      if (sp == 0 && w == 0 && ln == 0) __builtin_amdgcn_raw_buffer_store_b32(1u, rsW, 0, OFF_FL + (layer * 8 + kvh) * 128, AUX_SC1);
      STAMP(6 + 12 * layer);
      const float mm = u2f(mlv.x);
      float M = fmaxf(mm, dppf<XOR2>(mm));
      M = fmaxf(M, dppf<0x124>(M));
      M = fmaxf(M, dppf<ROR8>(M));
      M = xrow_max(M);
      const float wgt = __builtin_amdgcn_exp2f(mm - M);
      float L = wgt * u2f(mlv.y);
      L += dppf<XOR2>(L);
      L += dppf<0x124>(L);
      L += dppf<ROR8>(L);
      L = xrow_sum(L);
      const float fac = wgt * __builtin_amdgcn_rcpf(L);
      f32x4 o = {0.f, 0.f, 0.f, 0.f};
      const int src = (16 * w + ((ln >> 2) & 1)) * 4;
#pragma unroll
      for (int k = 0; k < 8; ++k) {
        const float f = u2f((u32)__builtin_amdgcn_ds_bpermute(src + 8 * k, (int)f2u(fac)));
        o += f * __builtin_bit_cast(f32x4, pa[k]);
      }
      f32x4* op = (f32x4*)(smem + L_MRG);
      op[w * 128 + ln] = o;
      STAMP(27 + layer);
      BAR();
      STAMP(29 + layer);
      const int pp = 16 * w + (ln >> 2);
      o = (op[pp] + op[128 + pp]) + (op[256 + pp] + op[384 + pp]);
      float a[8];
#pragma unroll
      for (int i = 0; i < 4; ++i) {
        const f32x2 t = (o.x * wof[0][i] + o.y * wof[1][i]) + (o.z * wof[2][i] + o.w * wof[3][i]);
        a[2 * i] = t.x; a[2 * i + 1] = t.y;
      }
#pragma unroll
      for (int i = 0; i < 4; ++i) a[i] = swap32_add(a[i], a[i + 4]);
#pragma unroll
      for (int i = 0; i < 2; ++i) {
        a[i] = swap16_add(a[i], a[i + 2]);
        a[i] += dppf<ROR8>(a[i]);
        a[i] += dppf<0x124>(a[i]);
      }
      float* const rx = (float*)(smem + L_RX);
      if ((ln & 12) == 0) {
        const int col = 8 * (ln & 3) + 4 * (ln >> 5) + 2 * ((ln >> 4) & 1);
        rx[col * 4 + w] = a[0];
        rx[col * 4 + 4 + w] = a[1];
      }
      BAR();
      if (w == 0 && ln < 32)
        __builtin_amdgcn_raw_buffer_store_b32(canon(f2u(sum4(*(const f32x4*)(rx + ln * 4)))), rsW, ln * 4, OFF_XB + (layer * 16 + rg) * 2048 + cs * 128, AUX_SC1);
    }
    STAMP(7 + 12 * layer);

    {
      v4u wt[4];
#pragma unroll
      for (int i = 0; i < 4; ++i) wt[i] = ldsW13[i * 256 + w * 64 + ln];
      f32x2 wf[2][8];
      unpack_w(wt, wf);
      float cs13 = sum4(*(const f32x4*)(red + 5 * RED_STRIDE + (ln & 15) * 4));
      f32x2 xi = ((const f32x2*)(smem + L_XI))[w * 64 + ln];
      asm volatile("" : "+v"(cs13), "+v"(xi));
      if (DLY_B) __builtin_amdgcn_s_sleep(DLY_B);
_Pragma("unroll 1")
      for (int spin = 0; spin < SPIN_CAP; ++spin) {
        CLOBBER();
        const u32 fl = __builtin_amdgcn_raw_buffer_load_b32(rsW, (ln & 7) * 128, OFF_FL + layer * 1024, AUX_SC1);
        if (__all(fl != SENT || (ln & 7) == kvh)) break;
      }
      v4u g[8];
_Pragma("unroll 1")
      for (int spin = 0; spin < SPIN_CAP; ++spin) {
        CLOBBER();
#pragma unroll
        for (int k = 0; k < 8; ++k) g[k] = ld128<AUX_SC1>(rsW, (ln & 31) * 16 + (ln >> 5) * 2048, OFF_XB + layer * 32768 + w * 512 + k * 4096);
        u32 mxw = 0;
#pragma unroll
        for (int k = 0; k < 8; ++k) mxw = umax(mxw, max4(g[k]));
        if (__all(mxw != SENT)) break;
        __builtin_amdgcn_s_sleep(DLY_RETRY);
      }
      STAMP(8 + 12 * layer);
      const f32x4 sm = ((__builtin_bit_cast(f32x4, g[0]) + __builtin_bit_cast(f32x4, g[1])) + (__builtin_bit_cast(f32x4, g[2]) + __builtin_bit_cast(f32x4, g[3]))) +
                       ((__builtin_bit_cast(f32x4, g[4]) + __builtin_bit_cast(f32x4, g[5])) + (__builtin_bit_cast(f32x4, g[6]) + __builtin_bit_cast(f32x4, g[7])));
      const float x1a = xi.x + swap32_add(sm.x, sm.z), x1b = xi.y + swap32_add(sm.y, sm.w);
      ln_gemv_part(x1a - cmean, x1b - cmean, wf, red + RED_STRIDE, w, ln);
      if (w * 128 + 4 * (ln & 31) + 2 * (ln >> 5) == dcol) *(f32x2*)(red + 3 * RED_STRIDE) = f32x2{x1a, x1b};
      BAR();
      float mu;
      const float gt = ln_gemv_final(red + RED_STRIDE, cs13, ln, mu);
      const float up = dppf<ROR8>(gt);
      const float hval = gt * __builtin_amdgcn_rcpf(1.0f + __expf(-gt)) * up;
      if (doS3 && w == 0 && ln < 8) __builtin_amdgcn_raw_buffer_store_b32(canon(f2u(hval)), rsW, ln * 4, OFF_H + layer * 8192 + s3 * 32, AUX_SC1);
      cmean = u2f((u32)__builtin_amdgcn_readfirstlane((int)f2u(cmean + mu)));
    }
    STAMP(9 + 12 * layer);
    if constexpr (layer == 0) { SCHED(); static_for<0, NPRE / 2>(issue2); SCHED(); }

    {
      f32x2 w2f[7];
#pragma unroll
      for (int k = 0; k < 7; ++k) {
        u32 t;
        if (k < 4) t = ((const u32*)(smem + L_W2))[k * 256 + w * 64 + ln];
        else { asm volatile("" : "+v"(w2r[k])); t = w2r[k]; }
        w2f[k] = f32x2{bflo(t), bfhi(t)};
        asm volatile("" : "+v"(w2f[k]));
      }
      __builtin_amdgcn_s_sleep(DLY_C1);
      u32 hv[7];
_Pragma("unroll 1")
      for (int spin = 0; spin < SPIN_CAP; ++spin) {
        CLOBBER();
        u32 mxw = 0;
#pragma unroll
        for (int k = 0; k < 7; ++k) hv[k] = __builtin_amdgcn_raw_buffer_load_b32(rsW, ln * 4, OFF_H + layer * 8192 + w * 256 + k * 1024, AUX_SC1);
#pragma unroll
        for (int k = 0; k < 7; ++k) mxw = umax(mxw, hv[k]);
        if (__all(mxw != SENT)) break;
        __builtin_amdgcn_s_sleep(DLY_RETRY);
      }
      STAMP(10 + 12 * layer);
      f32x2 a = u2f(hv[0]) * w2f[0];
#pragma unroll
      for (int k = 1; k < 7; ++k) a += u2f(hv[k]) * w2f[k];
      float r = swap32_add(a.x, a.y);
      r = swap16_add(r, r);
      r += dppf<XOR1>(r); r += dppf<XOR2>(r); r += dppf<HMIR>(r); r += dppf<MIR>(r);
      float* const rd = red + 2 * RED_STRIDE;
      if ((ln & 31) == 0) rd[(ln >> 5) * 4 + w] = r;
      BAR();
      if (w == 0 && ln == 0) {
        const f32x2 x1 = *(const f32x2*)(red + 3 * RED_STRIDE);
        const float y0 = x1.x + sum4(*(const f32x4*)rd), y1 = x1.y + sum4(*(const f32x4*)(rd + 4));
        if (layer == 0) __builtin_amdgcn_raw_buffer_store_b64(v2u{canon(f2u(y0)), canon(f2u(y1))}, rsW, 0, OFF_X2 + dcol * 4, AUX_SC1);
        else *(u32*)(p.out + dcol) = pack_bf16(y0, y1);
      }
    }
    STAMP(11 + 12 * layer);
    if constexpr (layer == 0) {
    SCHED(); static_for<NPRE / 2, NPRE>(issue2); SCHED();

    {
      v4u wt[4];
#pragma unroll
      for (int i = 0; i < 4; ++i) wt[i] = ldsWq[i * 256 + w * 64 + ln];
      f32x2 wf[2][8];
      unpack_w(wt, wf);
      float csq = sum4(*(const f32x4*)(red + 4 * RED_STRIDE + (ln & 15) * 4));
      asm volatile("" : "+v"(csq));
      __builtin_amdgcn_s_sleep(DLY_C2);
      v2u g;
_Pragma("unroll 1")
      for (int spin = 0; spin < SPIN_CAP; ++spin) {
        CLOBBER();
        g = __builtin_amdgcn_raw_buffer_load_b64(rsW, (ln & 31) * 16 + (ln >> 5) * 8, OFF_X2 + w * 512, AUX_SC1);
        if (__all(umax(g.x, g.y) != SENT)) break;
        __builtin_amdgcn_s_sleep(DLY_RETRY);
      }
      STAMP(12);
      const float xia = u2f(g.x), xib = u2f(g.y);
      ((v2u*)(smem + L_XI))[w * 64 + ln] = g;
      ln_gemv_part(xia - cmean, xib - cmean, wf, red, w, ln);
      BAR();
      float mu;
      const float qv = ln_gemv_final(red, csq, ln, mu);
      const u32 pk = canon(pack_bf16(qv, dppf<XOR1>(qv)));
      if (w == 0 && ln < 16 && (ln & 1) == 0) __builtin_amdgcn_raw_buffer_store_b32(pk, rsW, ln * 2, OFF_Q1 + qcol * 8, AUX_SC1);
      cmean = u2f((u32)__builtin_amdgcn_readfirstlane((int)f2u(cmean + mu)));
      STAMP(13);
    }
    int qo = voffK;
    asm volatile("" : "+v"(qo));
    qo = qoff(qo);
    __builtin_amdgcn_s_sleep(DLY_D);
_Pragma("unroll 1")
    for (int spin = 0; spin < SPIN_CAP; ++spin) {
      CLOBBER();
      qf = ld128<AUX_SC1>(rsW, qo, OFF_Q1 + kvh * 4096);
      if (__all(max4(qf) != SENT)) break;
    }
    }
  };
  layer_body(IC<0>{});
  layer_body(IC<1>{});
  asm volatile(".Ldk_code_end:");
}

extern "C" void kernel_launch(void* const* d_in, const int* in_sizes, int n_in, void* d_out, int out_size,
                              void* d_ws, size_t ws_size, hipStream_t stream) {
  Params p;
  p.x = (const u16*)d_in[0];
  p.K = (const u16*)d_in[1];
  p.V = (const u16*)d_in[2];
  p.wq = (const u16*)d_in[3];
  p.wo = (const u16*)d_in[4];
  p.w1 = (const u16*)d_in[6];
  p.w3 = (const u16*)d_in[7];
  p.w2 = (const u16*)d_in[8];
  p.out = (u16*)d_out;
  p.ws = (unsigned char*)d_ws;
  decode_kernel<<<256, 256, 0, stream>>>(p);
}
```

```cpp
#include <hip/hip_runtime.h>
#include <stdint.h>

typedef unsigned u32;
typedef unsigned long long u64;
typedef unsigned short u16;
typedef u32 v4u __attribute__((ext_vector_type(4)));
typedef u32 v2u __attribute__((ext_vector_type(2)));
typedef float f32x4 __attribute__((ext_vector_type(4)));
typedef float f32x2 __attribute__((ext_vector_type(2)));
typedef __bf16 bf16x8 __attribute__((ext_vector_type(8)));
typedef __bf16 bf16x2 __attribute__((ext_vector_type(2)));
typedef __amdgpu_buffer_rsrc_t rsrc_t;

#define KV_AUX 2
#define W_AUX 0
#define RING 10

constexpr int AUX_SC1 = 16;
constexpr u32 SENT = 0xFFFFFFFFu;
constexpr int SPIN_CAP = 200000;

constexpr int OFF_EPOCH = 0;
constexpr int OFF_READY = 4096;
constexpr int OFF_Q0 = 8192;
constexpr int OFF_HO = OFF_Q0 + 32768;
constexpr int PA_REC = 1152;
constexpr int OFF_PA = OFF_HO;
constexpr int OFF_XB = OFF_PA + 2 * 16 * 32 * PA_REC;
constexpr int OFF_H = OFF_XB + 2 * 16 * 2048;
constexpr int OFF_X2 = OFF_H + 2 * 8192;
constexpr int OFF_FL = OFF_X2 + 2048;
constexpr int OFF_Q1 = OFF_FL + 2048;
constexpr int HO_USED = OFF_Q1 + 32768 - OFF_HO;
constexpr int HO_SLICE = 5120;
static_assert(HO_USED <= 256 * HO_SLICE, "reset slices must cover the hand-off region");
constexpr int OFF_STAMP = 4 << 20;
constexpr int WS_BYTES = OFF_HO + 256 * HO_SLICE;

constexpr int RED_STRIDE = 80;
constexpr int L_RED = 0;
constexpr int L_ML = L_RED + 6 * RED_STRIDE * 4;
constexpr int L_MRG = L_ML + 128;
constexpr int L_RX = L_MRG + 1024;
constexpr int L_XI = L_MRG + 8192;
constexpr int L_W2 = L_XI + 2048;
constexpr int L_WQ = L_W2 + 4096;
constexpr int L_W13 = L_WQ + 16384;
constexpr int L_V = L_W13 + 16384;
constexpr int L_BYTES = L_V + 114688;
static_assert(L_BYTES <= 163840 && L_V % 16 == 0, "LDS");

#define DLY_A 12
#define DLY_B 0
#define DLY_C1 12
#define DLY_C2 12
#define DLY_D 5
#define DLY_RETRY 3

struct Params {
  const u16* x; const u16* K; const u16* V; const u16* wq; const u16* wo;
  const u16* w1; const u16* w3; const u16* w2; u16* out; unsigned char* ws;
};

__device__ __forceinline__ float u2f(u32 x) { return __builtin_bit_cast(float, x); }
__device__ __forceinline__ u32 f2u(float x) { return __builtin_bit_cast(u32, x); }
__device__ __forceinline__ float bflo(u32 x) { return u2f(x << 16); }
__device__ __forceinline__ float bfhi(u32 x) { return u2f(x & 0xffff0000u); }
__device__ __forceinline__ u32 pack_bf16(float lo, float hi) {
  bf16x2 r = {(__bf16)lo, (__bf16)hi};
  return __builtin_bit_cast(u32, r);
}
__device__ __forceinline__ u32 canon(u32 x) { return x == SENT ? 0x7FC07FC0u : x; }
template <int CTRL> __device__ __forceinline__ float dppf(float x) {
  return u2f((u32)__builtin_amdgcn_mov_dpp((int)f2u(x), CTRL, 0xf, 0xf, true));
}
template <int CTRL> __device__ __forceinline__ u32 dppu(u32 x) {
  return (u32)__builtin_amdgcn_mov_dpp((int)x, CTRL, 0xf, 0xf, true);
}
constexpr int XOR1 = 0xB1, XOR2 = 0x4E, XOR3 = 0x1B, HMIR = 0x141, MIR = 0x140, ROR8 = 0x128;
__device__ __forceinline__ float swap32_add(float a, float b) {
  auto r = __builtin_amdgcn_permlane32_swap(f2u(a), f2u(b), false, false);
  return u2f(r[0]) + u2f(r[1]);
}
__device__ __forceinline__ float swap16_add(float a, float b) {
  auto r = __builtin_amdgcn_permlane16_swap(f2u(a), f2u(b), false, false);
  return u2f(r[0]) + u2f(r[1]);
}
__device__ __forceinline__ float xrow_max(float x) {
  auto s = __builtin_amdgcn_permlane16_swap(f2u(x), f2u(x), false, false);
  x = fmaxf(u2f(s[0]), u2f(s[1]));
  auto t = __builtin_amdgcn_permlane32_swap(f2u(x), f2u(x), false, false);
  return fmaxf(u2f(t[0]), u2f(t[1]));
}
__device__ __forceinline__ float xrow_sum(float x) {
  x = swap16_add(x, x);
  return swap32_add(x, x);
}
__device__ __forceinline__ float wave_sum(float x) {
  x += dppf<XOR1>(x); x += dppf<XOR2>(x); x += dppf<HMIR>(x); x += dppf<MIR>(x);
  return xrow_sum(x);
}
__device__ __forceinline__ float treduce16(float (&v)[16], int lane) {
#pragma unroll
  for (int i = 0; i < 8; ++i) v[i] = swap32_add(v[i], v[i + 8]);
#pragma unroll
  for (int i = 0; i < 4; ++i) v[i] = swap16_add(v[i], v[i + 4]);
  const bool b3 = lane & 8, b2 = lane & 4;
#pragma unroll
  for (int i = 0; i < 2; ++i) {
    const float keep = b3 ? v[i + 2] : v[i], send = b3 ? v[i] : v[i + 2];
    v[i] = keep + dppf<ROR8>(send);
  }
  const float keep = b2 ? v[1] : v[0], send = b2 ? v[0] : v[1];
  float r = keep + dppf<HMIR>(send);
  r += dppf<XOR2>(r);
  r += dppf<XOR1>(r);
  return r;
}
__device__ __forceinline__ float treduce8(float (&v)[8], int lane) {
#pragma unroll
  for (int i = 0; i < 4; ++i) v[i] = swap32_add(v[i], v[i + 4]);
#pragma unroll
  for (int i = 0; i < 2; ++i) v[i] = swap16_add(v[i], v[i + 2]);
  const bool b3 = lane & 8;
  const float keep = b3 ? v[1] : v[0], send = b3 ? v[0] : v[1];
  float r = keep + dppf<ROR8>(send);
  r += dppf<XOR1>(r);
  r += dppf<XOR2>(r);
  r += dppf<HMIR>(r);
  return r;
}
__device__ __forceinline__ f32x4 mfma(v4u a, v4u b, f32x4 c) {
  return __builtin_amdgcn_mfma_f32_16x16x32_bf16(__builtin_bit_cast(bf16x8, a), __builtin_bit_cast(bf16x8, b), c, 0, 0, 0);
}
__device__ __forceinline__ f32x4 qk1(v4u k, v4u q) {
  f32x4 d;
  asm volatile("v_mfma_f32_16x16x32_bf16 %0, %1, %2, 0" : "=&v"(d) : "v"(k), "v"(q));
  return d;
}
__device__ __forceinline__ void qk_fence(f32x4 (&D)[4]) {
  asm volatile("s_nop 7" : "+v"(D[0]), "+v"(D[1]), "+v"(D[2]), "+v"(D[3]));
}
__device__ __forceinline__ void pv2a(f32x4& a0, f32x4& a1, v4u va, v4u vb, v4u pv) {
  asm volatile("s_nop 1\n\t"
      "v_mfma_f32_16x16x32_bf16 %0, %2, %4, %0\n\t"
      "v_mfma_f32_16x16x32_bf16 %1, %3, %4, %1"
      : "+v"(a0), "+v"(a1) : "a"(va), "a"(vb), "v"(pv));
}
__device__ __forceinline__ void pv2v(f32x4& a0, f32x4& a1, v4u va, v4u vb, v4u pv) {
  asm volatile("s_nop 1\n\t"
      "v_mfma_f32_16x16x32_bf16 %0, %2, %4, %0\n\t"
      "v_mfma_f32_16x16x32_bf16 %1, %3, %4, %1"
      : "+v"(a0), "+v"(a1) : "v"(va), "v"(vb), "v"(pv));
}
__device__ __forceinline__ v4u ldw(const void* base, u32 off) {
  const v4u* q = (const v4u*)((const unsigned char*)base + off);
  return *q;
}
constexpr int home(int T, int es) { return T >= 12 ? 2 : T == 11 ? (es < 2 ? 1 : 2) : (T % 3 == 2 ? 1 : 0); }
constexpr int hidx(int T, int es) { return T >= 12 ? T - 12 : T == 11 ? (es < 2 ? 3 : 4) : (T % 3 == 2 ? T / 3 : T - T / 3); }
constexpr int lslot(int T, int e) { return T == 11 ? 24 + (e >> 2) * 2 + (e & 3) : (T / 3) * 8 + e; }
template <int AUX> __device__ __forceinline__ v4u ld128(rsrc_t rs, int voff, int soff) {
  return __builtin_amdgcn_raw_buffer_load_b128(rs, voff, soff, AUX);
}
#define KEEPALL 0
#define ROTMUL 5
#define ROTMUL2 16
#define L2MOD 5
#define L2MASK 4
__device__ __forceinline__ int chunk(int j, int rot4k) { return (j * 4096 + rot4k) & 0x7FFFF; }
constexpr int NPRE = 8;
#define UNTOUCH 0
#define PROMOTE 0
#define MALLM 2
#define MALLN 1
#define CODEPF 3
#define PRE_MALL 1
#define PRE_EARLY 0
#define KEEP0 104
constexpr bool kept(int j) { return j >= KEEP0; }
constexpr int order2(int st) { return st == 0 ? 12 : st < 7 ? ((st & 1) ? st / 2 : 12 + st / 2) : st == 14 ? 11 : st == 15 ? 10 : st - 4; }
constexpr int blk2(int i) { return 8 * order2(i >> 3) + (i & 7); }
constexpr int hbm_before(int st) { int n = 0; for (int i = NPRE; i < 8 * st; ++i) n += kept(blk2(i)) ? 0 : 1; return n; }
template <bool FIRST> __device__ __forceinline__ v4u ldk(rsrc_t rs, int voff, int j, int rot4k) {
  if (FIRST && (kept(j) || (j < 96 && j % MALLM < MALLN) || (PRE_MALL && j >= 96))) return ld128<0>(rs, voff, chunk(j, rot4k));
  return ld128<KV_AUX>(rs, voff, chunk(j, rot4k));
}
#define W1 18
#define W2A 10
#define W2B 18
#define W2C 20
#define W2D 12
constexpr int win2(int i) { return i < 8 ? W2A : i < 24 ? W2B : i < 48 ? W2C : W2D; }
constexpr int cmin(int a, int b) { return a < b ? a : b; }
constexpr int cmax(int a, int b) { return a > b ? a : b; }
constexpr int hi_start(int st);
constexpr int hi_after(int i) { return i < 0 ? NPRE : cmax((i & 7) ? hi_after(i - 1) : hi_start(i >> 3), cmin(128, i + 1 + win2(i))); }
constexpr int hi_start(int st) { return cmax(hi_after(8 * st - 1), cmin(128, 8 * st + win2(8 * st))); }
__device__ __forceinline__ int qoff(int voffK) { return ((voffK >> 5) & 31) * 128 + (voffK & 31); }
__device__ __forceinline__ u32 umax(u32 a, u32 b) { return a > b ? a : b; }
__device__ __forceinline__ u32 max4(v4u v) { return umax(umax(v.x, v.y), umax(v.z, v.w)); }
#define BAR() asm volatile("s_waitcnt lgkmcnt(0)\n\ts_barrier" ::: "memory")
#define CLOBBER() asm volatile("" ::: "memory")
#define SCHED() __builtin_amdgcn_sched_barrier(0)
#define TILEBAR 1
#define PACE_OFF1 300
#define PACE_OFF2 2000
#define PACE_OFF3 0
#define PACE2 840
#define PACE1 0
#define PACE 920
__device__ __forceinline__ void pace(u32 t0, int due) {
  if (PACE) {
    asm volatile("1:\n\ts_memrealtime vcc\n\ts_waitcnt lgkmcnt(0)\n\ts_sub_u32 vcc_lo, vcc_lo, %0\n\t"
                 "s_cmp_lt_i32 vcc_lo, %1\n\ts_cbranch_scc1 1b" ::"s"(t0), "s"(due) : "vcc", "scc", "memory");
  }
  if (TILEBAR) __builtin_amdgcn_s_barrier();
}

#define STAMP(i) do { } while (0)

__device__ __forceinline__ void fma8(float* acc, float x, v4u w) {
  acc[0] = fmaf(x, bflo(w.x), acc[0]); acc[1] = fmaf(x, bfhi(w.x), acc[1]);
  acc[2] = fmaf(x, bflo(w.y), acc[2]); acc[3] = fmaf(x, bfhi(w.y), acc[3]);
  acc[4] = fmaf(x, bflo(w.z), acc[4]); acc[5] = fmaf(x, bfhi(w.z), acc[5]);
  acc[6] = fmaf(x, bflo(w.w), acc[6]); acc[7] = fmaf(x, bfhi(w.w), acc[7]);
}

__device__ __forceinline__ void unpack_w(const v4u (&wt)[4], f32x2 (&wf)[2][8]) {
#pragma unroll
  for (int r = 0; r < 2; ++r)
#pragma unroll
    for (int i = 0; i < 4; ++i) {
      wf[r][i] = f32x2{bflo(wt[r][i]), bfhi(wt[r][i])};
      wf[r][4 + i] = f32x2{bflo(wt[2 + r][i]), bfhi(wt[2 + r][i])};
    }
#pragma unroll
  for (int r = 0; r < 2; ++r)
#pragma unroll
    for (int i = 0; i < 8; ++i) asm volatile("" : "+v"(wf[r][i]));
}
__device__ __forceinline__ int red_slot(int w, int lane) {
  int o = (lane & 3) == 0 ? (lane + w) * 4 : (lane & 31) == 1 ? ((16 + (lane >> 5)) * 4 + w) * 4 : -1;
  asm volatile("" : "+v"(o));
  return o;
}
__device__ __forceinline__ void ln_gemv_part(float da, float db, const f32x2 (&wf)[2][8], float* red, int ro, int lane) {
  float a[16];
#pragma unroll
  for (int i = 0; i < 8; ++i) {
    const f32x2 t = da * wf[0][i] + db * wf[1][i];
    a[2 * i] = t.x; a[2 * i + 1] = t.y;
  }
  float st = swap32_add(da + db, fmaf(da, da, db * db));
  st = swap16_add(st, st);
  st += dppf<XOR1>(st); st += dppf<XOR2>(st); st += dppf<HMIR>(st); st += dppf<MIR>(st);
  const float r = treduce16(a, lane);
  float v = (lane & 1) ? st : r;
  asm volatile("" : "+v"(v));
  if (ro >= 0) *(float*)((unsigned char*)red + ro) = v;
}
__device__ __forceinline__ float sum4(f32x4 v) { return (v.x + v.y) + (v.z + v.w); }
__device__ __forceinline__ float ln_gemv_final(const float* red, float cs, int lane, float& mu) {
  const f32x4 g = *(const f32x4*)(red + (lane & 15) * 4), s1 = *(const f32x4*)(red + 64), s2 = *(const f32x4*)(red + 68);
  mu = sum4(s1) * (1.0f / 512.0f);
  const float var = fmaf(-mu, mu, sum4(s2) * (1.0f / 512.0f));
  return fmaf(-mu, cs, sum4(g)) * __builtin_amdgcn_rsqf(fmaxf(var, 0.f) + 1e-5f);
}

template <int I> struct IC { static constexpr int value = I; };
template <int I, int N, class F> __device__ __forceinline__ void static_for(F&& f) {
  if constexpr (I < N) { f(IC<I>{}); static_for<I + 1, N>(f); }
}

__global__ void __launch_bounds__(256, 1) decode_kernel(Params p) {
  __shared__ __attribute__((aligned(16))) unsigned char smem[L_BYTES];
  const int tid = threadIdx.x, lane = tid & 63, w = __builtin_amdgcn_readfirstlane(tid >> 6);
  const int b = blockIdx.x, kvh = b & 7, sp = b >> 3;
  u32 tp = PACE ? (u32)__builtin_amdgcn_s_memrealtime() : 0u;
  const rsrc_t rsW = __builtin_amdgcn_make_buffer_rsrc((void*)p.ws, 0, WS_BYTES, 0x00020000);
  const size_t kvoff = ((size_t)kvh * 65536 + (size_t)sp * 2048) * 128;
  const rsrc_t rsK = __builtin_amdgcn_make_buffer_rsrc((void*)(p.K + kvoff), 0, 2048 * 256, 0x00020000);
  const rsrc_t rsV = __builtin_amdgcn_make_buffer_rsrc((void*)(p.V + kvoff), 0, 2048 * 256, 0x00020000);

  float* const red = (float*)(smem + L_RED);
  u64 code0;
  u32 code_len;
  asm volatile("s_getpc_b64 %0\n.Ldk_code_begin:\n\ts_mov_b32 %1, .Ldk_code_end-.Ldk_code_begin" : "=s"(code0), "=s"(code_len));
  const rsrc_t rsC = __builtin_amdgcn_make_buffer_rsrc((void*)code0, 0, code_len, 0x00020000);

  const int rot4k = ((sp * ROTMUL + kvh * ROTMUL2) & 127) * 4096;
  int rotK1 = rot4k, rotK2 = rot4k;
  asm volatile("" : "+s"(rotK1));
  asm volatile("" : "+s"(rotK2));
  const int voffV = lane * 16 + w * 1024;
  const int voffK = ((lane >> 2) & 3) * 256 + ((lane >> 4) * 4 + (lane & 3)) * 16 + w * 1024;
  v4u vresA[8][8], vresV[5][8];
  v4u raw[3][8], rawL[2][8];
  v4u k1[128], k2[128];
#pragma unroll
  for (int T = 0; T < 3; ++T)
#pragma unroll
    for (int t = 0; t < 8; ++t) raw[T][t] = ld128<KV_AUX>(rsV, voffV, chunk(8 * T + t, rot4k));
  SCHED();
  u32 touch[6];
  touch[0] = *(const u32*)((const unsigned char*)p.wq + (u32)((16 * sp + 4 * w + (lane >> 4)) * 8192 + kvh * 1024 + (lane & 15) * 64));
  touch[3] = CODEPF ? __builtin_amdgcn_raw_buffer_load_b32(rsC, tid * 64, sp * 16384, 0) : 0u;
  if (w == 0) {
    const v4u s4 = {SENT, SENT, SENT, SENT};
#pragma unroll
    for (int i = 0; i < 5; ++i) __builtin_amdgcn_raw_buffer_store_b128(s4, rsW, OFF_HO + lane * 16 + i * 1024, b * HO_SLICE, AUX_SC1);
  }
  const v2u ep = __builtin_amdgcn_raw_buffer_load_b64(rsW, OFF_EPOCH, 0, AUX_SC1);
  const int row0 = w * 128 + 4 * (lane & 31) + 2 * (lane >> 5);
  const int qcol = kvh * 512 + sp * 16;
  const int rg = 2 * kvh + (sp & 1), cs = sp >> 1;
  const bool doS3 = sp < 28;
  const int s3 = kvh * 28 + (doS3 ? sp : 0);
  const int dcol = kvh * 64 + sp * 2;
  u32 x01 = 0, x00 = 0;
  SCHED();

  float cmean = 0.f;
  u32 tag_lo = 0, tag_hi = 0;
  u32 w2r[7];
  v4u qf = {0u, 0u, 0u, 0u}, r0 = qf, r1 = qf;
  v4u wtmp[8], wot[4];
  v4u* const ldsV = (v4u*)(smem + L_V) + w * (28 * 64) + lane;
  v4u* const ldsWq = (v4u*)(smem + L_WQ);
  v4u* const ldsW13 = (v4u*)(smem + L_W13);
  auto ingest = [&](auto TC, v4u (&rw)[8], auto&& between) __attribute__((always_inline)) {
    constexpr int T = decltype(TC)::value;
    v4u f[8];
#pragma unroll
    for (int e = 0; e < 8; ++e)
#pragma unroll
      for (int j = 0; j < 4; ++j)
        f[e][j] = __builtin_amdgcn_perm(rw[j + 4][e >> 1], rw[j][e >> 1], (e & 1) ? 0x07060302u : 0x05040100u);
#pragma unroll
    for (int e = 0; e < 8; ++e)
      if (home(T, e & 3) == 2) asm volatile("" : "+v"(f[e]));
    SCHED();
    between();
#pragma unroll
    for (int e = 0; e < 8; ++e) {
      if (home(T, e & 3) == 1) ldsV[lslot(T, e) * 64] = f[e];
      else if (home(T, e & 3) == 2) vresV[hidx(T, e & 3)][e] = f[e];
      else { asm volatile("" : "+a"(f[e])); vresA[hidx(T, e & 3)][e] = f[e]; }
    }
    SCHED();
  };
  auto issue1 = [&](auto UC) __attribute__((always_inline)) {
    constexpr int u = decltype(UC)::value;
    if constexpr (u < 112) k1[u] = ldk<true>(rsK, voffK, u, rotK1);
    else if constexpr (u < 120) rawL[0][u - 112] = ld128<KV_AUX>(rsV, voffV, chunk(u, rot4k));
    else if constexpr (u < 128) k1[u - 8] = ldk<true>(rsK, voffK, u - 8, rotK1);
    else if constexpr (u < 136) rawL[1][u - 128] = ld128<KV_AUX>(rsV, voffV, chunk(u - 8, rot4k));
    else if constexpr (u < 144) k1[u - 16] = ldk<true>(rsK, voffK, u - 16, rotK1);
  };
  static_for<0, 14>([&](auto TC) __attribute__((always_inline)) {
    constexpr int T = decltype(TC)::value;
    ingest(TC, raw[T % 3], [&]() __attribute__((always_inline)) {
    pace(tp, T * PACE1 / 8 + PACE_OFF1);
    SCHED();
    if constexpr (T + 3 < 14) {
#pragma unroll
      for (int t = 0; t < 8; ++t) raw[T % 3][t] = ld128<KV_AUX>(rsV, voffV, chunk(8 * (T + 3) + t, rot4k));
    } else {
      static_for<(T - 11) * W1 / 3, (T - 10) * W1 / 3>(issue1);
    }
    if constexpr (T == 0) {
      const int l7 = lane < 56 ? lane : 55;
      touch[1] = *(const u32*)((const unsigned char*)(w < 2 ? p.w1 : p.w3) + (u32)((16 * sp + 8 * (w & 1) + l7 / 7) * 3584 + kvh * 448 + (l7 % 7) * 64));
    }
    if constexpr (T == 1) {
      x01 = *(const u32*)(p.x + row0);
      x00 = *(const u32*)p.x;
      const int l2 = lane < 28 ? lane : 27;
      touch[2] = *(const u32*)((const unsigned char*)p.w2 + (u32)((56 * sp + 14 * w + (l2 >> 1)) * 1024 + kvh * 128 + (l2 & 1) * 64));
    }
    if constexpr (T == 1 || T == 2) {
#pragma unroll
      for (int i = 2 * (T - 1); i < 2 * (T - 1) + 2; ++i) wtmp[i] = ldw(p.wq, (w * 128 + 32 * i + (lane >> 1)) * 8192 + qcol * 2 + (lane & 1) * 16);
    }
    if constexpr (T >= 3 && T < 7) wtmp[T + 1] = ldw(T < 5 ? p.w1 : p.w3, (row0 + ((T + 1) & 1)) * 3584 + s3 * 16);
    if constexpr (T >= 5 && T < 12) w2r[T - 5] = *(const u32*)((const unsigned char*)p.w2 + (u32)((tid + 256 * (T - 5)) * 1024 + dcol * 2));
    if constexpr (T >= 7 && T < 11) {
      const int pp = 16 * w + (lane >> 2);
      const int orow = rg * 256 + ((pp >> 2) & 1) * 128 + ((pp >> 3) & 3) * 32 + (pp >> 5) * 4 + (pp & 3);
      wot[T - 7] = ldw(p.wo, (orow + 8 * (T - 7)) * 1024 + cs * 64 + (lane & 3) * 16);
    }
    if constexpr (T == 5) touch[4] = __builtin_amdgcn_raw_buffer_load_b32(rsK, lane * 8192, 0, KV_AUX);
    if constexpr (T == 9) touch[5] = *(const u32*)(p.out + 2 * lane);
    if constexpr (T >= 8 && T < 12) ((u32*)(smem + L_W2))[(T - 8) * 256 + tid] = w2r[T - 8];
    if constexpr (T == 5) asm volatile("" ::"v"(touch[0]), "v"(touch[1]), "v"(touch[2]), "v"(touch[3]));
    if constexpr (T == 13) asm volatile("" ::"v"(touch[4]), "v"(touch[5]));
    });
    if constexpr (T == 0) STAMP(24);
    if constexpr (T == 5) {
#pragma unroll
      for (int i = 0; i < 4; ++i) {
        const int rho = 32 * i + (lane >> 1);
        ldsWq[((lane & 1) * 2 + (rho & 1)) * 256 + w * 64 + (rho >> 2) + 32 * ((rho >> 1) & 1)] = wtmp[i];
      }
      v4u wt[4];
#pragma unroll
      for (int i = 0; i < 4; ++i) wt[i] = ldsWq[i * 256 + tid];
      f32x2 wf[2][8];
      unpack_w(wt, wf);
      const float xia = bflo(x01), xib = bfhi(x01);
      ((f32x2*)(smem + L_XI))[tid] = f32x2{xia, xib};
      cmean = bflo(x00);
      const int ro = red_slot(w, lane);
      ln_gemv_part(1.0f, 1.0f, wf, red + 4 * RED_STRIDE, ro, lane);
      ln_gemv_part(xia - cmean, xib - cmean, wf, red, ro, lane);
      BAR();
      const float csq = sum4(*(const f32x4*)(red + 4 * RED_STRIDE + (lane & 15) * 4));
      float mu;
      const float qv = ln_gemv_final(red, csq, lane, mu);
      cmean = u2f((u32)__builtin_amdgcn_readfirstlane((int)f2u(cmean + mu)));
      const u32 pk = canon(pack_bf16(qv, dppf<XOR1>(qv)));
      if (w == 0 && lane < 16 && (lane & 1) == 0) __builtin_amdgcn_raw_buffer_store_b32(pk, rsW, lane * 2, OFF_Q0 + qcol * 8, AUX_SC1);
      const u64 epoch = (((u64)ep.y << 32) | ep.x) + 0x9E3779B97F4A7C15ull;
      tag_lo = (u32)__builtin_amdgcn_readfirstlane((int)(u32)epoch);
      tag_hi = (u32)__builtin_amdgcn_readfirstlane((int)(u32)(epoch >> 32));
      STAMP(1);
      SCHED();
    }
    if constexpr (T == 7) if (w == 0) {
      asm volatile("s_waitcnt vmcnt(16)" ::: "memory");
      if (lane == 0) __builtin_amdgcn_raw_buffer_store_b64(v2u{tag_lo, tag_hi}, rsW, 0, OFF_READY + b * 8, AUX_SC1);
      SCHED();
    }
    if constexpr (T == 9) {
      const v4u wt[4] = {wtmp[4], wtmp[5], wtmp[6], wtmp[7]};
#pragma unroll
      for (int i = 0; i < 4; ++i) ldsW13[i * 256 + tid] = wt[i];
      f32x2 wf[2][8];
      unpack_w(wt, wf);
      ln_gemv_part(1.0f, 1.0f, wf, red + 5 * RED_STRIDE, red_slot(w, lane), lane);
      SCHED();
    }
    if constexpr (T == 11) {
      r0 = ld128<AUX_SC1>(rsW, OFF_READY + lane * 16, 0);
      r1 = ld128<AUX_SC1>(rsW, OFF_READY + 1024 + lane * 16, 0);
      SCHED();
    }
    if constexpr (T == 13) {
      auto not_ready = [&](v4u a, v4u c) {
        const u32 bad = ((a.x ^ tag_lo) | (a.y ^ tag_hi) | (a.z ^ tag_lo) | (a.w ^ tag_hi)) |
                        ((c.x ^ tag_lo) | (c.y ^ tag_hi) | (c.z ^ tag_lo) | (c.w ^ tag_hi));
        return __any(bad != 0);
      };
      if (__builtin_expect(not_ready(r0, r1), 0)) {
_Pragma("unroll 1")
        for (int spin = 0; spin < SPIN_CAP; ++spin) {
          __builtin_amdgcn_s_sleep(8);
          CLOBBER();
          const v4u a = ld128<AUX_SC1>(rsW, OFF_READY + lane * 16, 0), c = ld128<AUX_SC1>(rsW, OFF_READY + 1024 + lane * 16, 0);
          if (!not_ready(a, c)) break;
        }
      }
      CLOBBER();
      qf = ld128<AUX_SC1>(rsW, qoff(voffK), OFF_Q0 + kvh * 4096);
      if (b == 0 && tid == 0) __builtin_amdgcn_raw_buffer_store_b64(v2u{tag_lo, tag_hi}, rsW, OFF_EPOCH, 0, AUX_SC1);
      SCHED();
    }
  });
  STAMP(2);

  const int c4 = lane & 3;
  const bool cb0 = lane & 1, cb1 = lane & 2;
  constexpr float C2 = 0.08838834764831845f * 1.4426950408889634f;

  auto issue2 = [&](auto IC_) __attribute__((always_inline)) {
    constexpr int i = decltype(IC_)::value;
    k2[blk2(i)] = ldk<false>(rsK, voffK, blk2(i), rotK2);
  };
  auto layer_body = [&](auto LC) __attribute__((always_inline)) {
    constexpr int layer = decltype(LC)::value;
    STAMP(3 + 12 * layer);
    if (PACE && layer) tp = (u32)__builtin_amdgcn_s_memrealtime();
    constexpr int tbase = layer ? PACE_OFF3 : PACE_OFF2, tstep = layer ? PACE2 / 8 : PACE / 8;
    float m = -INFINITY, mc = 0.f, l = 0.f;
    f32x4 acc[2] = {f32x4{0.f, 0.f, 0.f, 0.f}, f32x4{0.f, 0.f, 0.f, 0.f}};
    u32 junk[4] = {0u, 0u, 0u, 0u};
    int ln = lane;
    asm volatile("s_nop 1" : "+v"(qf));
    static_for<0, 16>([&](auto SC) __attribute__((always_inline)) {
      constexpr int st = decltype(SC)::value;
      constexpr int T = layer ? order2(st) : st;
      if constexpr (st > 0 && st < 15) { constexpr int due = layer ? hbm_before(st) * PACE2 / 64 + tbase : st * tstep + tbase; pace(tp, due); SCHED(); }
      if constexpr (layer == 1) { static_for<hi_after(8 * st - 1), hi_start(st)>(issue2); SCHED(); }
      if constexpr (layer == 0 && T >= 14)
        ingest(IC<T>{}, rawL[T - 14], [&]() __attribute__((always_inline)) { static_for<112 + 16 * (T - 14) + W1, 120 + 16 * (T - 14) + W1>(issue1); });
      if constexpr (layer == 0 && UNTOUCH && st == 2) {
        const int l7 = lane < 56 ? lane : 55, l2 = lane < 28 ? lane : 27;
        junk[0] = __builtin_nontemporal_load((const u32*)((const unsigned char*)p.wq + (u32)((16 * sp + 4 * w + (lane >> 4)) * 8192 + kvh * 1024 + (lane & 15) * 64)));
        junk[1] = __builtin_nontemporal_load((const u32*)((const unsigned char*)(w < 2 ? p.w1 : p.w3) + (u32)((16 * sp + 8 * (w & 1) + l7 / 7) * 3584 + kvh * 448 + (l7 % 7) * 64)));
        junk[2] = __builtin_nontemporal_load((const u32*)((const unsigned char*)p.w2 + (u32)((56 * sp + 14 * w + (l2 >> 1)) * 1024 + kvh * 128 + (l2 & 1) * 64)));
        SCHED();
      }
      if constexpr (layer == 0 && UNTOUCH && st == 5) { asm volatile("" ::"v"(junk[0]), "v"(junk[1]), "v"(junk[2])); SCHED(); }
      if constexpr (layer == 0 && (CODEPF & 2) && st == 13) { junk[3] = __builtin_amdgcn_raw_buffer_load_b32(rsC, tid * 64, sp * 16384, AUX_SC1); SCHED(); }
      if constexpr (layer == 0 && (CODEPF & 2) && st == 15) { asm volatile("" ::"v"(junk[3])); SCHED(); }
      float X[2];
      static_for<0, 2>([&](auto HC) __attribute__((always_inline)) {
        constexpr int hf = decltype(HC)::value;
        f32x4 D[4];
        static_for<0, 4>([&](auto QC) __attribute__((always_inline)) {
          constexpr int t = decltype(QC)::value, blk = 8 * T + 4 * hf + t, i = 8 * st + 4 * hf + t;
          D[t] = qk1(layer ? k2[blk] : k1[blk], qf);
          if constexpr (layer == 0) issue1(IC<(blk < 112 ? blk : blk < 120 ? blk + 8 : blk + 16) + W1>{});
          else static_for<((i & 7) ? hi_after(i - 1) : hi_start(st)), hi_after(i)>(issue2);
          SCHED();
        });
        qk_fence(D);
        SCHED();
        const float e0 = cb1 ? (cb0 ? D[3][3] : D[2][2]) : (cb0 ? D[1][1] : D[0][0]);
        const float e1 = cb1 ? (cb0 ? D[2][3] : D[3][2]) : (cb0 ? D[0][1] : D[1][0]);
        const float e2 = cb1 ? (cb0 ? D[1][3] : D[0][2]) : (cb0 ? D[3][1] : D[2][0]);
        const float e3 = cb1 ? (cb0 ? D[0][3] : D[1][2]) : (cb0 ? D[2][1] : D[3][0]);
        X[hf] = (e0 + dppf<XOR1>(e1)) + (dppf<XOR2>(e2) + dppf<XOR3>(e3));
      });
      if constexpr (layer == 0 && PROMOTE && kept(8 * T)) {
        junk[T & 3] = __builtin_amdgcn_raw_buffer_load_b32(rsK, ((8 * T + (lane >> 3)) * 4096 + rotK1 & 0x7FFFF) + w * 1024 + (lane & 7) * 128, 0, AUX_SC1);
        SCHED();
      }
      float mx = fmaxf(X[0], X[1]);
      if (__any(mx > m)) {
        mx = fmaxf(mx, dppf<XOR1>(mx));
        mx = fmaxf(mx, dppf<XOR2>(mx));
        mx = xrow_max(mx);
        const float mn = fmaxf(m, mx);
        const float mcn = (mn == -INFINITY) ? 0.f : mn * C2;
        const float alpha = __builtin_amdgcn_exp2f(m * C2 - mcn);
        l *= alpha;
        acc[0] *= alpha; acc[1] *= alpha;
        m = mn; mc = mcn;
      }
      const float p0 = __builtin_amdgcn_exp2f(fmaf(X[0], C2, -mc)), p1 = __builtin_amdgcn_exp2f(fmaf(X[1], C2, -mc));
      l += p0 + p1;
      const u32 Y = pack_bf16(p0, p1);
      const v4u Yb = {dppu<0x00>(Y), dppu<0x55>(Y), dppu<0xAA>(Y), dppu<0xFF>(Y)};
#pragma unroll
      for (int es = 0; es < 4; ++es) {
        const bool on = c4 == es;
        const v4u pv = {on ? Yb.x : 0u, on ? Yb.y : 0u, on ? Yb.z : 0u, on ? Yb.w : 0u};
        if (home(T, es) == 0) pv2a(acc[0], acc[1], vresA[hidx(T, es)][es], vresA[hidx(T, es)][4 + es], pv);
        else if (home(T, es) == 2) pv2v(acc[0], acc[1], vresV[hidx(T, es)][es], vresV[hidx(T, es)][4 + es], pv);
        else pv2v(acc[0], acc[1], ldsV[lslot(T, es) * 64], ldsV[lslot(T, 4 + es) * 64], pv);
      }
      SCHED();
    });
    STAMP(4 + 12 * layer);
    asm volatile("" : "+v"(ln));

    l += dppf<XOR1>(l); l += dppf<XOR2>(l); l = xrow_sum(l);
    asm volatile("s_nop 7" : "+v"(acc[0]), "+v"(acc[1]));
    {
      f32x4* mrg = (f32x4*)(smem + L_MRG);
      mrg[(w * 2 + 0) * 64 + ln] = acc[0];
      mrg[(w * 2 + 1) * 64 + ln] = acc[1];
      f32x2* ml = (f32x2*)(smem + L_ML);
      if (ln < 16 && (ln & 3) == 0) ml[(ln >> 2) * 4 + w] = f32x2{mc, l};
      BAR();
      if (w < 2) {
        const int h = (ln >> 2) & 3;
        const f32x4 qa = *(const f32x4*)(ml + h * 4), qb = *(const f32x4*)(ml + h * 4 + 2);
        const float M = fmaxf(fmaxf(qa.x, qa.z), fmaxf(qb.x, qb.z));
        const float f0 = __builtin_amdgcn_exp2f(qa.x - M), f1 = __builtin_amdgcn_exp2f(qa.z - M);
        const float f2 = __builtin_amdgcn_exp2f(qb.x - M), f3 = __builtin_amdgcn_exp2f(qb.z - M);
        const f32x4 o = (f0 * mrg[w * 64 + ln] + f1 * mrg[(2 + w) * 64 + ln]) + (f2 * mrg[(4 + w) * 64 + ln] + f3 * mrg[(6 + w) * 64 + ln]);
        const float L = fmaf(f0, qa.y, f1 * qa.w) + fmaf(f2, qb.y, f3 * qb.w);
        const int pos = (w * 32 + (ln >> 4) * 8 + (h & 1) * 4 + (ln & 3)) * 16 + (h >> 1) * (32 * PA_REC);
        const int rec = OFF_PA + ((layer * 16 + 2 * kvh) * 32 + sp) * PA_REC;
        const v4u ov = {canon(f2u(o.x)), canon(f2u(o.y)), canon(f2u(o.z)), canon(f2u(o.w))};
        __builtin_amdgcn_raw_buffer_store_b128(ov, rsW, pos, rec, AUX_SC1);
        if (w == 0 && ln < 16 && (ln & 3) == 0)
          __builtin_amdgcn_raw_buffer_store_b64(v2u{canon(f2u(M)), canon(f2u(L))}, rsW, 1024 + (h & 1) * 8 + (h >> 1) * (32 * PA_REC), rec, AUX_SC1);
      }
    }
    STAMP(5 + 12 * layer);

    {
      if (w >= 2) __builtin_amdgcn_s_sleep(8);
      if (DLY_A) __builtin_amdgcn_s_sleep(DLY_A);
      v4u pa[8];
      v2u mlv;
      const int base = OFF_PA + (layer * 16 + rg) * 32 * PA_REC;
      CLOBBER();
      mlv = __builtin_amdgcn_raw_buffer_load_b64(rsW, 1024 + (ln >> 1) * PA_REC + (ln & 1) * 8, base, AUX_SC1);
#pragma unroll
      for (int k = 0; k < 8; ++k) pa[k] = ld128<AUX_SC1>(rsW, ln * 16, base + (8 * w + k) * PA_REC);
      SCHED();
      if constexpr (layer == 0 && PROMOTE) asm volatile("" ::"v"(junk[0]), "v"(junk[1]), "v"(junk[2]), "v"(junk[3]));
      int rxo = ((8 * (ln & 3) + 4 * (ln >> 5) + 2 * ((ln >> 4) & 1)) * 4 + w) * 4, ppo = (16 * w + (ln >> 2)) * 16;
      asm volatile("" : "+v"(rxo), "+v"(ppo));
      f32x2 wof[4][4];
#pragma unroll
      for (int r = 0; r < 4; ++r)
#pragma unroll
        for (int i = 0; i < 4; ++i) { if (i == 0) asm volatile("" : "+v"(wot[r])); wof[r][i] = f32x2{bflo(wot[r][i]), bfhi(wot[r][i])}; asm volatile("" : "+v"(wof[r][i])); }
      SCHED();
_Pragma("unroll 1")
      for (int spin = 0; spin < SPIN_CAP; ++spin) {
        u32 mk[8], mxw = umax(mlv.x, mlv.y);
#pragma unroll
        for (int k = 0; k < 8; ++k) { mk[k] = max4(pa[k]); mxw = umax(mxw, mk[k]); }
        if (__all(mxw != SENT)) break;
        u32 need = __any(umax(mlv.x, mlv.y) == SENT) ? 0x100u : 0u;
#pragma unroll
        for (int k = 0; k < 8; ++k) need |= __any(mk[k] == SENT) ? (1u << k) : 0u;
        CLOBBER();
        if (need & 0x100) mlv = __builtin_amdgcn_raw_buffer_load_b64(rsW, 1024 + (ln >> 1) * PA_REC + (ln & 1) * 8, base, AUX_SC1);
#pragma unroll
        for (int k = 0; k < 8; ++k)
          if (need & (1 << k)) pa[k] = ld128<AUX_SC1>(rsW, ln * 16, base + (8 * w + k) * PA_REC);
        SCHED();
      }
      __builtin_amdgcn_s_waitcnt(0x0F70);
      if (sp == 0 && w == 0 && ln == 0) __builtin_amdgcn_raw_buffer_store_b32(1u, rsW, 0, OFF_FL + (layer * 8 + kvh) * 128, AUX_SC1);
      STAMP(6 + 12 * layer);
      const float mm = u2f(mlv.x);
      float M = fmaxf(mm, dppf<XOR2>(mm));
      M = fmaxf(M, dppf<0x124>(M));
      M = fmaxf(M, dppf<ROR8>(M));
      M = xrow_max(M);
      const float wgt = __builtin_amdgcn_exp2f(mm - M);
      float L = wgt * u2f(mlv.y);
      L += dppf<XOR2>(L);
      L += dppf<0x124>(L);
      L += dppf<ROR8>(L);
      L = xrow_sum(L);
      const float fac = wgt * __builtin_amdgcn_rcpf(L);
      f32x4 o = {0.f, 0.f, 0.f, 0.f};
      const int src = (16 * w + ((ln >> 2) & 1)) * 4;
      float fk[8];
#pragma unroll
      for (int k = 0; k < 8; ++k) fk[k] = u2f((u32)__builtin_amdgcn_ds_bpermute(src + 8 * k, (int)f2u(fac)));
      SCHED();
#pragma unroll
      for (int k = 0; k < 8; ++k) o += fk[k] * __builtin_bit_cast(f32x4, pa[k]);
      f32x4* op = (f32x4*)(smem + L_MRG);
      op[w * 128 + ln] = o;
      STAMP(27 + layer);
      BAR();
      STAMP(29 + layer);
      const f32x4* opp = (const f32x4*)(smem + L_MRG + ppo);
      o = (opp[0] + opp[128]) + (opp[256] + opp[384]);
      float a[8];
#pragma unroll
      for (int i = 0; i < 4; ++i) {
        const f32x2 t = (o.x * wof[0][i] + o.y * wof[1][i]) + (o.z * wof[2][i] + o.w * wof[3][i]);
        a[2 * i] = t.x; a[2 * i + 1] = t.y;
      }
#pragma unroll
      for (int i = 0; i < 4; ++i) a[i] = swap32_add(a[i], a[i + 4]);
#pragma unroll
      for (int i = 0; i < 2; ++i) {
        a[i] = swap16_add(a[i], a[i + 2]);
        a[i] += dppf<ROR8>(a[i]);
        a[i] += dppf<0x124>(a[i]);
      }
      float* const rx = (float*)(smem + L_RX);
      if ((ln & 12) == 0) {
        float* const rp = (float*)(smem + L_RX + rxo);
        rp[0] = a[0];
        rp[4] = a[1];
      }
      BAR();
      if (w == 0 && ln < 32)
        __builtin_amdgcn_raw_buffer_store_b32(canon(f2u(sum4(*(const f32x4*)(rx + ln * 4)))), rsW, ln * 4, OFF_XB + (layer * 16 + rg) * 2048 + cs * 128, AUX_SC1);
    }
    STAMP(7 + 12 * layer);

    {
      v4u wt[4];
#pragma unroll
      for (int i = 0; i < 4; ++i) wt[i] = ldsW13[i * 256 + w * 64 + ln];
      f32x2 wf[2][8];
      unpack_w(wt, wf);
      float cs13 = sum4(*(const f32x4*)(red + 5 * RED_STRIDE + (ln & 15) * 4));
      f32x2 xi = ((const f32x2*)(smem + L_XI))[w * 64 + ln];
      const int ro = red_slot(w, ln);
      int own = w * 128 + 4 * (ln & 31) + 2 * (ln >> 5) - dcol;
      asm volatile("" : "+v"(cs13), "+v"(xi), "+v"(own));
      if (DLY_B) __builtin_amdgcn_s_sleep(DLY_B);
_Pragma("unroll 1")
      for (int spin = 0; spin < SPIN_CAP; ++spin) {
        CLOBBER();
        const u32 fl = __builtin_amdgcn_raw_buffer_load_b32(rsW, (ln & 7) * 128, OFF_FL + layer * 1024, AUX_SC1);
        if (__all(fl != SENT || (ln & 7) == kvh)) break;
      }
      v4u g[8];
_Pragma("unroll 1")
      for (int spin = 0; spin < SPIN_CAP; ++spin) {
        CLOBBER();
#pragma unroll
        for (int k = 0; k < 8; ++k) g[k] = ld128<AUX_SC1>(rsW, (ln & 31) * 16 + (ln >> 5) * 2048, OFF_XB + layer * 32768 + w * 512 + k * 4096);
        u32 mxw = 0;
#pragma unroll
        for (int k = 0; k < 8; ++k) mxw = umax(mxw, max4(g[k]));
        if (__all(mxw != SENT)) break;
        __builtin_amdgcn_s_sleep(DLY_RETRY);
      }
      STAMP(8 + 12 * layer);
      const f32x4 sm = ((__builtin_bit_cast(f32x4, g[0]) + __builtin_bit_cast(f32x4, g[1])) + (__builtin_bit_cast(f32x4, g[2]) + __builtin_bit_cast(f32x4, g[3]))) +
                       ((__builtin_bit_cast(f32x4, g[4]) + __builtin_bit_cast(f32x4, g[5])) + (__builtin_bit_cast(f32x4, g[6]) + __builtin_bit_cast(f32x4, g[7])));
      const float x1a = xi.x + swap32_add(sm.x, sm.z), x1b = xi.y + swap32_add(sm.y, sm.w);
      if (own == 0) *(f32x2*)(red + 3 * RED_STRIDE) = f32x2{x1a, x1b};
      ln_gemv_part(x1a - cmean, x1b - cmean, wf, red + RED_STRIDE, ro, ln);
      BAR();
      float mu;
      const float gt = ln_gemv_final(red + RED_STRIDE, cs13, ln, mu);
      const float up = dppf<ROR8>(gt);
      const float hval = gt * __builtin_amdgcn_rcpf(1.0f + __expf(-gt)) * up;
      if (doS3 && w == 0 && ln < 8) __builtin_amdgcn_raw_buffer_store_b32(canon(f2u(hval)), rsW, ln * 4, OFF_H + layer * 8192 + s3 * 32, AUX_SC1);
      cmean = u2f((u32)__builtin_amdgcn_readfirstlane((int)f2u(cmean + mu)));
    }
    STAMP(9 + 12 * layer);

    if constexpr (layer == 0 && PRE_EARLY) { SCHED(); static_for<0, NPRE / 2>(issue2); SCHED(); }
    {
      f32x2 w2f[7];
#pragma unroll
      for (int k = 0; k < 7; ++k) {
        u32 t;
        if (k < 4) t = ((const u32*)(smem + L_W2))[k * 256 + w * 64 + ln];
        else { asm volatile("" : "+v"(w2r[k])); t = w2r[k]; }
        w2f[k] = f32x2{bflo(t), bfhi(t)};
        asm volatile("" : "+v"(w2f[k]));
      }
      if constexpr (layer == 0 && !PRE_EARLY) { SCHED(); static_for<0, NPRE / 2>(issue2); SCHED(); }
      __builtin_amdgcn_s_sleep(DLY_C1);
      u32 hv[7];
_Pragma("unroll 1")
      for (int spin = 0; spin < SPIN_CAP; ++spin) {
        CLOBBER();
        u32 mxw = 0;
#pragma unroll
        for (int k = 0; k < 7; ++k) hv[k] = __builtin_amdgcn_raw_buffer_load_b32(rsW, ln * 4, OFF_H + layer * 8192 + w * 256 + k * 1024, AUX_SC1);
#pragma unroll
        for (int k = 0; k < 7; ++k) mxw = umax(mxw, hv[k]);
        if (__all(mxw != SENT)) break;
        __builtin_amdgcn_s_sleep(DLY_RETRY);
      }
      STAMP(10 + 12 * layer);
      f32x2 a = u2f(hv[0]) * w2f[0];
#pragma unroll
      for (int k = 1; k < 7; ++k) a += u2f(hv[k]) * w2f[k];
      float r = swap32_add(a.x, a.y);
      r = swap16_add(r, r);
      r += dppf<XOR1>(r); r += dppf<XOR2>(r); r += dppf<HMIR>(r); r += dppf<MIR>(r);
      float* const rd = red + 2 * RED_STRIDE;
      if ((ln & 31) == 0) rd[(ln >> 5) * 4 + w] = r;
      BAR();
      if (w == 0 && ln == 0) {
        const f32x2 x1 = *(const f32x2*)(red + 3 * RED_STRIDE);
        const float y0 = x1.x + sum4(*(const f32x4*)rd), y1 = x1.y + sum4(*(const f32x4*)(rd + 4));
        if (layer == 0) __builtin_amdgcn_raw_buffer_store_b64(v2u{canon(f2u(y0)), canon(f2u(y1))}, rsW, 0, OFF_X2 + dcol * 4, AUX_SC1);
        else *(u32*)(p.out + dcol) = pack_bf16(y0, y1);
      }
    }
    STAMP(11 + 12 * layer);
    if constexpr (layer == 0) {

    if constexpr (PRE_EARLY) { SCHED(); static_for<NPRE / 2, NPRE>(issue2); SCHED(); }
    {
      v4u wt[4];
#pragma unroll
      for (int i = 0; i < 4; ++i) wt[i] = ldsWq[i * 256 + w * 64 + ln];
      f32x2 wf[2][8];
      unpack_w(wt, wf);
      float csq = sum4(*(const f32x4*)(red + 4 * RED_STRIDE + (ln & 15) * 4));
      asm volatile("" : "+v"(csq));
      const int ro = red_slot(w, ln);
      if constexpr (!PRE_EARLY) { SCHED(); static_for<NPRE / 2, NPRE>(issue2); SCHED(); }
      __builtin_amdgcn_s_sleep(DLY_C2);
      v2u g;
_Pragma("unroll 1")
      for (int spin = 0; spin < SPIN_CAP; ++spin) {
        CLOBBER();
        g = __builtin_amdgcn_raw_buffer_load_b64(rsW, (ln & 31) * 16 + (ln >> 5) * 8, OFF_X2 + w * 512, AUX_SC1);
        if (__all(umax(g.x, g.y) != SENT)) break;
        __builtin_amdgcn_s_sleep(DLY_RETRY);
      }
      STAMP(12);
      const float xia = u2f(g.x), xib = u2f(g.y);
      ((v2u*)(smem + L_XI))[w * 64 + ln] = g;
      ln_gemv_part(xia - cmean, xib - cmean, wf, red, ro, ln);
      BAR();
      float mu;
      const float qv = ln_gemv_final(red, csq, ln, mu);
      const u32 pk = canon(pack_bf16(qv, dppf<XOR1>(qv)));
      if (w == 0 && ln < 16 && (ln & 1) == 0) __builtin_amdgcn_raw_buffer_store_b32(pk, rsW, ln * 2, OFF_Q1 + qcol * 8, AUX_SC1);
      cmean = u2f((u32)__builtin_amdgcn_readfirstlane((int)f2u(cmean + mu)));
      STAMP(13);
    }
    int qo = voffK;
    asm volatile("" : "+v"(qo));
    qo = qoff(qo);
    __builtin_amdgcn_s_sleep(DLY_D);
_Pragma("unroll 1")
    for (int spin = 0; spin < SPIN_CAP; ++spin) {
      CLOBBER();
      qf = ld128<AUX_SC1>(rsW, qo, OFF_Q1 + kvh * 4096);
      if (__all(max4(qf) != SENT)) break;
    }
    }
  };
  layer_body(IC<0>{});
  layer_body(IC<1>{});
  asm volatile(".Ldk_code_end:");
}

extern "C" void kernel_launch(void* const* d_in, const int* in_sizes, int n_in, void* d_out, int out_size,
                              void* d_ws, size_t ws_size, hipStream_t stream) {
  Params p;
  p.x = (const u16*)d_in[0];
  p.K = (const u16*)d_in[1];
  p.V = (const u16*)d_in[2];
  p.wq = (const u16*)d_in[3];
  p.wo = (const u16*)d_in[4];
  p.w1 = (const u16*)d_in[6];
  p.w3 = (const u16*)d_in[7];
  p.w2 = (const u16*)d_in[8];
  p.out = (u16*)d_out;
  p.ws = (unsigned char*)d_ws;
  decode_kernel<<<256, 256, 0, stream>>>(p);
}
```

```cpp
#include <hip/hip_runtime.h>
#include <stdint.h>

typedef unsigned u32;
typedef unsigned long long u64;
typedef unsigned short u16;
typedef u32 v4u __attribute__((ext_vector_type(4)));
typedef u32 v2u __attribute__((ext_vector_type(2)));
typedef float f32x4 __attribute__((ext_vector_type(4)));
typedef float f32x2 __attribute__((ext_vector_type(2)));
typedef __bf16 bf16x2 __attribute__((ext_vector_type(2)));
typedef __amdgpu_buffer_rsrc_t rsrc_t;

constexpr int AUX_NT = 2;
constexpr int AUX_SC1 = 16;
constexpr u32 SENT = 0xFFFFFFFFu;
constexpr int SPIN_CAP = 200000;

constexpr int OFF_EPOCH = 0;
constexpr int OFF_READY = 4096;
constexpr int OFF_Q0 = 8192;
constexpr int OFF_HO = OFF_Q0 + 32768;
constexpr int PA_REC = 1152;
constexpr int OFF_PA = OFF_HO;
constexpr int OFF_XB = OFF_PA + 2 * 16 * 32 * PA_REC;
constexpr int OFF_H = OFF_XB + 2 * 16 * 2048;
constexpr int OFF_X2 = OFF_H + 2 * 8192;
constexpr int OFF_FL = OFF_X2 + 2048;
constexpr int OFF_Q1 = OFF_FL + 2048;
constexpr int HO_USED = OFF_Q1 + 32768 - OFF_HO;
constexpr int HO_SLICE = 5120;
static_assert(HO_USED <= 256 * HO_SLICE, "reset slices must cover the hand-off region");
constexpr int WS_BYTES = OFF_HO + 256 * HO_SLICE;

constexpr int RED_STRIDE = 80;
constexpr int L_RED = 0;
constexpr int L_ML = L_RED + 6 * RED_STRIDE * 4;
constexpr int L_MRG = L_ML + 128;
constexpr int L_RX = L_MRG + 1024;
constexpr int L_XI = L_MRG + 8192;
constexpr int L_W2 = L_XI + 2048;
constexpr int L_WQ = L_W2 + 4096;
constexpr int L_W13 = L_WQ + 16384;
constexpr int L_V = L_W13 + 16384;
constexpr int L_BYTES = L_V + 114688;
static_assert(L_BYTES <= 163840 && L_V % 16 == 0, "LDS");

constexpr int DLY_A = 16;
constexpr int DLY_C1 = 12;
constexpr int DLY_C2 = 12;
constexpr int DLY_D = 5;
constexpr int DLY_RETRY = 3;

struct Params {
  const u16* x; const u16* K; const u16* V; const u16* wq; const u16* wo;
  const u16* w1; const u16* w3; const u16* w2; u16* out; unsigned char* ws;
};

__device__ __forceinline__ float u2f(u32 x) { return __builtin_bit_cast(float, x); }
__device__ __forceinline__ u32 f2u(float x) { return __builtin_bit_cast(u32, x); }
__device__ __forceinline__ float bflo(u32 x) { return u2f(x << 16); }
__device__ __forceinline__ float bfhi(u32 x) { return u2f(x & 0xffff0000u); }
__device__ __forceinline__ u32 pack_bf16(float lo, float hi) {
  bf16x2 r = {(__bf16)lo, (__bf16)hi};
  return __builtin_bit_cast(u32, r);
}
__device__ __forceinline__ u32 canon(u32 x) { return x < SENT - 1u ? x : SENT - 1u; }
template <int CTRL> __device__ __forceinline__ float dppf(float x) {
  return u2f((u32)__builtin_amdgcn_mov_dpp((int)f2u(x), CTRL, 0xf, 0xf, true));
}
template <int CTRL> __device__ __forceinline__ u32 dppu(u32 x) {
  return (u32)__builtin_amdgcn_mov_dpp((int)x, CTRL, 0xf, 0xf, true);
}
constexpr int XOR1 = 0xB1, XOR2 = 0x4E, XOR3 = 0x1B, HMIR = 0x141, MIR = 0x140, ROR8 = 0x128;
__device__ __forceinline__ float swap32_add(float a, float b) {
  auto r = __builtin_amdgcn_permlane32_swap(f2u(a), f2u(b), false, false);
  return u2f(r[0]) + u2f(r[1]);
}
__device__ __forceinline__ float swap16_add(float a, float b) {
  auto r = __builtin_amdgcn_permlane16_swap(f2u(a), f2u(b), false, false);
  return u2f(r[0]) + u2f(r[1]);
}
__device__ __forceinline__ float xrow_max(float x) {
  auto s = __builtin_amdgcn_permlane16_swap(f2u(x), f2u(x), false, false);
  x = fmaxf(u2f(s[0]), u2f(s[1]));
  auto t = __builtin_amdgcn_permlane32_swap(f2u(x), f2u(x), false, false);
  return fmaxf(u2f(t[0]), u2f(t[1]));
}
__device__ __forceinline__ float xrow_sum(float x) {
  x = swap16_add(x, x);
  return swap32_add(x, x);
}
__device__ __forceinline__ float treduce16(float (&v)[16], int lane) {
#pragma unroll
  for (int i = 0; i < 8; ++i) v[i] = swap32_add(v[i], v[i + 8]);
#pragma unroll
  for (int i = 0; i < 4; ++i) v[i] = swap16_add(v[i], v[i + 4]);
  const bool b3 = lane & 8, b2 = lane & 4;
#pragma unroll
  for (int i = 0; i < 2; ++i) {
    const float keep = b3 ? v[i + 2] : v[i], send = b3 ? v[i] : v[i + 2];
    v[i] = keep + dppf<ROR8>(send);
  }
  const float keep = b2 ? v[1] : v[0], send = b2 ? v[0] : v[1];
  float r = keep + dppf<HMIR>(send);
  r += dppf<XOR2>(r);
  r += dppf<XOR1>(r);
  return r;
}
__device__ __forceinline__ f32x4 qk1(v4u k, v4u q) {
  f32x4 d;
  asm volatile("v_mfma_f32_16x16x32_bf16 %0, %1, %2, 0" : "=&v"(d) : "v"(k), "v"(q));
  return d;
}
__device__ __forceinline__ void qk_fence(f32x4 (&D)[4]) {
  asm volatile("s_nop 7" : "+v"(D[0]), "+v"(D[1]), "+v"(D[2]), "+v"(D[3]));
}
__device__ __forceinline__ void pv2a(f32x4& a0, f32x4& a1, v4u va, v4u vb, v4u pv) {
  asm volatile("s_nop 1\n\t"
      "v_mfma_f32_16x16x32_bf16 %0, %2, %4, %0\n\t"
      "v_mfma_f32_16x16x32_bf16 %1, %3, %4, %1"
      : "+v"(a0), "+v"(a1) : "a"(va), "a"(vb), "v"(pv));
}
__device__ __forceinline__ void pv2v(f32x4& a0, f32x4& a1, v4u va, v4u vb, v4u pv) {
  asm volatile("s_nop 1\n\t"
      "v_mfma_f32_16x16x32_bf16 %0, %2, %4, %0\n\t"
      "v_mfma_f32_16x16x32_bf16 %1, %3, %4, %1"
      : "+v"(a0), "+v"(a1) : "v"(va), "v"(vb), "v"(pv));
}
__device__ __forceinline__ v4u ldw(const void* base, u32 off) {
  return *(const v4u*)((const unsigned char*)base + off);
}
constexpr int home(int T, int es) { return T >= 12 ? 2 : T == 11 ? (es < 2 ? 1 : 2) : (T % 3 == 2 ? 1 : 0); }
constexpr int hidx(int T, int es) { return T >= 12 ? T - 12 : T == 11 ? (es < 2 ? 3 : 4) : (T % 3 == 2 ? T / 3 : T - T / 3); }
constexpr int lslot(int T, int e) { return T == 11 ? 24 + (e >> 2) * 2 + (e & 3) : (T / 3) * 8 + e; }
template <int AUX> __device__ __forceinline__ v4u ld128(rsrc_t rs, int voff, int soff) {
  return __builtin_amdgcn_raw_buffer_load_b128(rs, voff, soff, AUX);
}
__device__ __forceinline__ int chunk(int j, int rot4k) { return (j * 4096 + rot4k) & 0x7FFFF; }
constexpr int RD = 3;
constexpr int MALLM = 3;
constexpr int MALLN = 1;
constexpr int NPRE = 8;
constexpr int order2(int st) { return st == 0 ? 12 : st < 7 ? ((st & 1) ? st / 2 : 12 + st / 2) : st == 14 ? 11 : st == 15 ? 10 : st - 4; }
constexpr int blk2(int i) { return 8 * order2(i >> 3) + (i & 7); }
template <bool FIRST> __device__ __forceinline__ v4u ldk(rsrc_t rs, int voff, int j, int rot4k) {
  if (FIRST && (j >= 96 || j % MALLM < MALLN)) return ld128<0>(rs, voff, chunk(j, rot4k));
  return ld128<AUX_NT>(rs, voff, chunk(j, rot4k));
}
constexpr int W1 = 18;
constexpr int win2(int i) { return i < 8 ? 10 : i < 24 ? 18 : i < 48 ? 20 : 12; }
constexpr int cmin(int a, int b) { return a < b ? a : b; }
constexpr int cmax(int a, int b) { return a > b ? a : b; }
constexpr int hi_start(int st);
constexpr int hi_after(int i) { return i < 0 ? NPRE : cmax((i & 7) ? hi_after(i - 1) : hi_start(i >> 3), cmin(128, i + 1 + win2(i))); }
constexpr int hi_start(int st) { return cmax(hi_after(8 * st - 1), cmin(128, 8 * st + win2(8 * st))); }
__device__ __forceinline__ int qoff(int voffK) { return ((voffK >> 5) & 31) * 128 + (voffK & 31); }
__device__ __forceinline__ u32 umax(u32 a, u32 b) { return a > b ? a : b; }
__device__ __forceinline__ u32 max4(v4u v) { return umax(umax(v.x, v.y), umax(v.z, v.w)); }
#define BAR() asm volatile("s_waitcnt lgkmcnt(0)\n\ts_barrier" ::: "memory")
#define CLOBBER() asm volatile("" ::: "memory")
#define SCHED() __builtin_amdgcn_sched_barrier(0)
__device__ __forceinline__ void unpack_w(const v4u (&wt)[4], f32x2 (&wf)[2][8]) {
#pragma unroll
  for (int r = 0; r < 2; ++r)
#pragma unroll
    for (int i = 0; i < 4; ++i) {
      wf[r][i] = f32x2{bflo(wt[r][i]), bfhi(wt[r][i])};
      wf[r][4 + i] = f32x2{bflo(wt[2 + r][i]), bfhi(wt[2 + r][i])};
    }
#pragma unroll
  for (int r = 0; r < 2; ++r)
#pragma unroll
    for (int i = 0; i < 8; ++i) asm volatile("" : "+v"(wf[r][i]));
}
__device__ __forceinline__ int red_slot(int w, int lane) {
  int o = (lane & 3) == 0 ? (lane + w) * 4 : (lane & 31) == 1 ? ((16 + (lane >> 5)) * 4 + w) * 4 : -1;
  asm volatile("" : "+v"(o));
  return o;
}
__device__ __forceinline__ void ln_gemv_part(float da, float db, const f32x2 (&wf)[2][8], float* red, int ro, int lane) {
  float a[16];
#pragma unroll
  for (int i = 0; i < 8; ++i) {
    const f32x2 t = da * wf[0][i] + db * wf[1][i];
    a[2 * i] = t.x; a[2 * i + 1] = t.y;
  }
  float st = swap32_add(da + db, fmaf(da, da, db * db));
  st = swap16_add(st, st);
  st += dppf<XOR1>(st); st += dppf<XOR2>(st); st += dppf<HMIR>(st); st += dppf<MIR>(st);
  const float r = treduce16(a, lane);
  float v = (lane & 1) ? st : r;
  asm volatile("" : "+v"(v));
  if (ro >= 0) *(float*)((unsigned char*)red + ro) = v;
}
__device__ __forceinline__ float sum4(f32x4 v) { return (v.x + v.y) + (v.z + v.w); }
__device__ __forceinline__ float ln_gemv_final(const float* red, float cs, int lane, float& mu) {
  const f32x4 g = *(const f32x4*)(red + (lane & 15) * 4), s1 = *(const f32x4*)(red + 64), s2 = *(const f32x4*)(red + 68);
  mu = sum4(s1) * (1.0f / 512.0f);
  const float var = fmaf(-mu, mu, sum4(s2) * (1.0f / 512.0f));
  return fmaf(-mu, cs, sum4(g)) * __builtin_amdgcn_rsqf(fmaxf(var, 0.f) + 1e-5f);
}

template <int I> struct IC { static constexpr int value = I; };
template <int I, int N, class F> __device__ __forceinline__ void static_for(F&& f) {
  if constexpr (I < N) { f(IC<I>{}); static_for<I + 1, N>(f); }
}

__global__ void __launch_bounds__(256, 1) decode_kernel(Params p) {
  __shared__ __attribute__((aligned(16))) unsigned char smem[L_BYTES];
  const int tid = threadIdx.x, lane = tid & 63, w = __builtin_amdgcn_readfirstlane(tid >> 6);
  const int b = blockIdx.x, kvh = b & 7, sp = b >> 3;
  const rsrc_t rsW = __builtin_amdgcn_make_buffer_rsrc((void*)p.ws, 0, WS_BYTES, 0x00020000);
  const size_t kvoff = ((size_t)kvh * 65536 + (size_t)sp * 2048) * 128;
  const rsrc_t rsK = __builtin_amdgcn_make_buffer_rsrc((void*)(p.K + kvoff), 0, 2048 * 256, 0x00020000);
  const rsrc_t rsV = __builtin_amdgcn_make_buffer_rsrc((void*)(p.V + kvoff), 0, 2048 * 256, 0x00020000);

  float* const red = (float*)(smem + L_RED);
  u64 code0;
  u32 code_len;
  asm volatile("s_getpc_b64 %0\n.Ldk_code_begin:\n\ts_mov_b32 %1, .Ldk_code_end-.Ldk_code_begin" : "=s"(code0), "=s"(code_len));
  const rsrc_t rsC = __builtin_amdgcn_make_buffer_rsrc((void*)code0, 0, code_len, 0x00020000);

  const int rot4k = ((sp * 5 + kvh * 16) & 127) * 4096;
  int rotK1 = rot4k, rotK2 = rot4k;
  asm volatile("" : "+s"(rotK1));
  asm volatile("" : "+s"(rotK2));
  const int voffV = lane * 16 + w * 1024;
  const int voffK = ((lane >> 2) & 3) * 256 + ((lane >> 4) * 4 + (lane & 3)) * 16 + w * 1024;
  v4u vresA[8][8], vresV[5][8];
  v4u raw[RD][8], rawL[2][8];
  v4u k1[128], k2[128];
#pragma unroll
  for (int T = 0; T < RD; ++T)
#pragma unroll
    for (int t = 0; t < 8; ++t) raw[T][t] = ld128<AUX_NT>(rsV, voffV, chunk(8 * T + t, rot4k));
  SCHED();
  u32 touch[6];
  touch[0] = *(const u32*)((const unsigned char*)p.wq + (u32)((16 * sp + 4 * w + (lane >> 4)) * 8192 + kvh * 1024 + (lane & 15) * 64));
  touch[3] = __builtin_amdgcn_raw_buffer_load_b32(rsC, tid * 64, sp * 16384, 0);
  if (w == 0) {
    const v4u s4 = {SENT, SENT, SENT, SENT};
#pragma unroll
    for (int i = 0; i < 5; ++i) __builtin_amdgcn_raw_buffer_store_b128(s4, rsW, OFF_HO + lane * 16 + i * 1024, b * HO_SLICE, AUX_SC1);
  }
  const v2u ep = __builtin_amdgcn_raw_buffer_load_b64(rsW, OFF_EPOCH, 0, AUX_SC1);
  const int row0 = w * 128 + 4 * (lane & 31) + 2 * (lane >> 5);
  const int qcol = kvh * 512 + sp * 16;
  const int rg = 2 * kvh + (sp & 1), cs = sp >> 1;
  const bool doS3 = sp < 28;
  const int s3 = kvh * 28 + (doS3 ? sp : 0);
  const int dcol = kvh * 64 + sp * 2;
  u32 x01 = 0, x00 = 0;
  SCHED();

  float cmean = 0.f;
  u32 tag_lo = 0, tag_hi = 0;
  u32 w2r[7];
  v4u qf = {0u, 0u, 0u, 0u}, r0 = qf, r1 = qf;
  v4u wtmp[8], wot[4];
  v4u* const ldsV = (v4u*)(smem + L_V) + w * (28 * 64) + lane;
  v4u* const ldsWq = (v4u*)(smem + L_WQ);
  v4u* const ldsW13 = (v4u*)(smem + L_W13);
  auto ingest = [&](auto TC, v4u (&rw)[8], auto&& between) __attribute__((always_inline)) {
    constexpr int T = decltype(TC)::value;
    v4u f[8];
#pragma unroll
    for (int e = 0; e < 8; ++e)
#pragma unroll
      for (int j = 0; j < 4; ++j)
        f[e][j] = __builtin_amdgcn_perm(rw[j + 4][e >> 1], rw[j][e >> 1], (e & 1) ? 0x07060302u : 0x05040100u);
#pragma unroll
    for (int e = 0; e < 8; ++e)
      if (home(T, e & 3) == 2) asm volatile("" : "+v"(f[e]));
    SCHED();
    between();
#pragma unroll
    for (int e = 0; e < 8; ++e) {
      if (home(T, e & 3) == 1) ldsV[lslot(T, e) * 64] = f[e];
      else if (home(T, e & 3) == 2) vresV[hidx(T, e & 3)][e] = f[e];
      else { asm volatile("" : "+a"(f[e])); vresA[hidx(T, e & 3)][e] = f[e]; }
    }
    SCHED();
  };
  auto issue1 = [&](auto UC) __attribute__((always_inline)) {
    constexpr int u = decltype(UC)::value;
    if constexpr (u < 112) k1[u] = ldk<true>(rsK, voffK, u, rotK1);
    else if constexpr (u < 120) rawL[0][u - 112] = ld128<AUX_NT>(rsV, voffV, chunk(u, rot4k));
    else if constexpr (u < 128) k1[u - 8] = ldk<true>(rsK, voffK, u - 8, rotK1);
    else if constexpr (u < 136) rawL[1][u - 128] = ld128<AUX_NT>(rsV, voffV, chunk(u - 8, rot4k));
    else if constexpr (u < 144) k1[u - 16] = ldk<true>(rsK, voffK, u - 16, rotK1);
  };
  static_for<0, 14>([&](auto TC) __attribute__((always_inline)) {
    constexpr int T = decltype(TC)::value;
    ingest(TC, raw[T % RD], [&]() __attribute__((always_inline)) {
    __builtin_amdgcn_s_barrier();
    SCHED();
    if constexpr (T + RD < 14) {
#pragma unroll
      for (int t = 0; t < 8; ++t) raw[T % RD][t] = ld128<AUX_NT>(rsV, voffV, chunk(8 * (T + RD) + t, rot4k));
    } else {
      static_for<(T - 14 + RD) * W1 / RD, (T - 13 + RD) * W1 / RD>(issue1);
    }
    if constexpr (T == 0) {
      const int l7 = lane < 56 ? lane : 55;
      touch[1] = *(const u32*)((const unsigned char*)(w < 2 ? p.w1 : p.w3) + (u32)((16 * sp + 8 * (w & 1) + l7 / 7) * 3584 + kvh * 448 + (l7 % 7) * 64));
    }
    if constexpr (T == 1) {
      x01 = *(const u32*)(p.x + row0);
      x00 = *(const u32*)p.x;
      const int l2 = lane < 28 ? lane : 27;
      touch[2] = *(const u32*)((const unsigned char*)p.w2 + (u32)((56 * sp + 14 * w + (l2 >> 1)) * 1024 + kvh * 128 + (l2 & 1) * 64));
    }
    if constexpr (T == 1 || T == 2) {
#pragma unroll
      for (int i = 2 * (T - 1); i < 2 * (T - 1) + 2; ++i) wtmp[i] = ldw(p.wq, (w * 128 + 32 * i + (lane >> 1)) * 8192 + qcol * 2 + (lane & 1) * 16);
    }
    if constexpr (T >= 3 && T < 7) wtmp[T + 1] = ldw(T < 5 ? p.w1 : p.w3, (row0 + ((T + 1) & 1)) * 3584 + s3 * 16);
    if constexpr (T >= 5 && T < 12) w2r[T - 5] = *(const u32*)((const unsigned char*)p.w2 + (u32)((tid + 256 * (T - 5)) * 1024 + dcol * 2));
    if constexpr (T >= 7 && T < 11) {
      const int pp = 16 * w + (lane >> 2);
      const int orow = rg * 256 + ((pp >> 2) & 1) * 128 + ((pp >> 3) & 3) * 32 + (pp >> 5) * 4 + (pp & 3);
      wot[T - 7] = ldw(p.wo, (orow + 8 * (T - 7)) * 1024 + cs * 64 + (lane & 3) * 16);
    }
    if constexpr (T == 5) touch[4] = __builtin_amdgcn_raw_buffer_load_b32(rsK, (lane & 1) * 0x7FF80, 0, AUX_NT);
    if constexpr (T == 9) touch[5] = *(const u32*)(p.out + 2 * lane);
    if constexpr (T >= 8 && T < 12) ((u32*)(smem + L_W2))[(T - 8) * 256 + tid] = w2r[T - 8];
    if constexpr (T == 5) asm volatile("" ::"v"(touch[0]), "v"(touch[1]), "v"(touch[2]), "v"(touch[3]));
    if constexpr (T == 13) asm volatile("" ::"v"(touch[4]), "v"(touch[5]));
    });
    if constexpr (T == 5) {
#pragma unroll
      for (int i = 0; i < 4; ++i) {
        const int rho = 32 * i + (lane >> 1);
        ldsWq[((lane & 1) * 2 + (rho & 1)) * 256 + w * 64 + (rho >> 2) + 32 * ((rho >> 1) & 1)] = wtmp[i];
      }
      v4u wt[4];
#pragma unroll
      for (int i = 0; i < 4; ++i) wt[i] = ldsWq[i * 256 + tid];
      f32x2 wf[2][8];
      unpack_w(wt, wf);
      const float xia = bflo(x01), xib = bfhi(x01);
      ((f32x2*)(smem + L_XI))[tid] = f32x2{xia, xib};
      cmean = bflo(x00);
      const int ro = red_slot(w, lane);
      ln_gemv_part(1.0f, 1.0f, wf, red + 4 * RED_STRIDE, ro, lane);
      ln_gemv_part(xia - cmean, xib - cmean, wf, red, ro, lane);
      BAR();
      const float csq = sum4(*(const f32x4*)(red + 4 * RED_STRIDE + (lane & 15) * 4));
      float mu;
      const float qv = ln_gemv_final(red, csq, lane, mu);
      cmean = u2f((u32)__builtin_amdgcn_readfirstlane((int)f2u(cmean + mu)));
      const u32 pk = canon(pack_bf16(qv, dppf<XOR1>(qv)));
      if (w == 0 && lane < 16 && (lane & 1) == 0) __builtin_amdgcn_raw_buffer_store_b32(pk, rsW, lane * 2, OFF_Q0 + qcol * 8, AUX_SC1);
      const u64 epoch = (((u64)ep.y << 32) | ep.x) + 0x9E3779B97F4A7C15ull;
      tag_lo = (u32)__builtin_amdgcn_readfirstlane((int)(u32)epoch);
      tag_hi = (u32)__builtin_amdgcn_readfirstlane((int)(u32)(epoch >> 32));
      SCHED();
    }
    if constexpr (T == 7) if (w == 0) {
      asm volatile("s_waitcnt vmcnt(16)" ::: "memory");
      if (lane == 0) __builtin_amdgcn_raw_buffer_store_b64(v2u{tag_lo, tag_hi}, rsW, 0, OFF_READY + b * 8, AUX_SC1);
      SCHED();
    }
    if constexpr (T == 9) {
      const v4u wt[4] = {wtmp[4], wtmp[5], wtmp[6], wtmp[7]};
#pragma unroll
      for (int i = 0; i < 4; ++i) ldsW13[i * 256 + tid] = wt[i];
      f32x2 wf[2][8];
      unpack_w(wt, wf);
      ln_gemv_part(1.0f, 1.0f, wf, red + 5 * RED_STRIDE, red_slot(w, lane), lane);
      SCHED();
    }
    if constexpr (T == 11) {
      r0 = ld128<AUX_SC1>(rsW, OFF_READY + lane * 16, 0);
      r1 = ld128<AUX_SC1>(rsW, OFF_READY + 1024 + lane * 16, 0);
      SCHED();
    }
    if constexpr (T == 13) {
      auto not_ready = [&](v4u a, v4u c) {
        const u32 bad = ((a.x ^ tag_lo) | (a.y ^ tag_hi) | (a.z ^ tag_lo) | (a.w ^ tag_hi)) |
                        ((c.x ^ tag_lo) | (c.y ^ tag_hi) | (c.z ^ tag_lo) | (c.w ^ tag_hi));
        return __any(bad != 0);
      };
      if (__builtin_expect(not_ready(r0, r1), 0)) {
_Pragma("unroll 1")
        for (int spin = 0; spin < SPIN_CAP; ++spin) {
          __builtin_amdgcn_s_sleep(8);
          CLOBBER();
          const v4u a = ld128<AUX_SC1>(rsW, OFF_READY + lane * 16, 0), c = ld128<AUX_SC1>(rsW, OFF_READY + 1024 + lane * 16, 0);
          if (!not_ready(a, c)) break;
        }
      }
      CLOBBER();
      qf = ld128<AUX_SC1>(rsW, qoff(voffK), OFF_Q0 + kvh * 4096);
      if (b == 0 && tid == 0) __builtin_amdgcn_raw_buffer_store_b64(v2u{tag_lo, tag_hi}, rsW, OFF_EPOCH, 0, AUX_SC1);
      SCHED();
    }
  });

  const int c4 = lane & 3;
  const bool cb0 = lane & 1, cb1 = lane & 2;
  constexpr float C2 = 0.08838834764831845f * 1.4426950408889634f;

  auto issue2 = [&](auto IC_) __attribute__((always_inline)) {
    constexpr int i = decltype(IC_)::value;
    k2[blk2(i)] = ldk<false>(rsK, voffK, blk2(i), rotK2);
  };
  auto layer_body = [&](auto LC) __attribute__((always_inline)) {
    constexpr int layer = decltype(LC)::value;
    float m = -INFINITY, mc = 0.f, l = 0.f;
    f32x4 acc[2] = {f32x4{0.f, 0.f, 0.f, 0.f}, f32x4{0.f, 0.f, 0.f, 0.f}};
    u32 junk = 0u;
    int ln = lane;
    asm volatile("s_nop 1" : "+v"(qf));
    static_for<0, 16>([&](auto SC) __attribute__((always_inline)) {
      constexpr int st = decltype(SC)::value;
      constexpr int T = layer ? order2(st) : st;
      if constexpr (st > 0 && st < 15) { __builtin_amdgcn_s_barrier(); SCHED(); }
      if constexpr (layer == 1) { static_for<hi_after(8 * st - 1), hi_start(st)>(issue2); SCHED(); }
      if constexpr (layer == 0 && T >= 14)
        ingest(IC<T>{}, rawL[T - 14], [&]() __attribute__((always_inline)) { static_for<112 + 16 * (T - 14) + W1, 120 + 16 * (T - 14) + W1>(issue1); });
      if constexpr (layer == 0 && st == 13) { junk = __builtin_amdgcn_raw_buffer_load_b32(rsC, tid * 64, sp * 16384, AUX_SC1); SCHED(); }
      if constexpr (layer == 0 && st == 15) { asm volatile("" ::"v"(junk)); SCHED(); }
      float X[2];
      static_for<0, 2>([&](auto HC) __attribute__((always_inline)) {
        constexpr int hf = decltype(HC)::value;
        f32x4 D[4];
        static_for<0, 4>([&](auto QC) __attribute__((always_inline)) {
          constexpr int t = decltype(QC)::value, blk = 8 * T + 4 * hf + t, i = 8 * st + 4 * hf + t;
          D[t] = qk1(layer ? k2[blk] : k1[blk], qf);
          if constexpr (layer == 0) issue1(IC<(blk < 112 ? blk : blk < 120 ? blk + 8 : blk + 16) + W1>{});
          else static_for<((i & 7) ? hi_after(i - 1) : hi_start(st)), hi_after(i)>(issue2);
          SCHED();
        });
        qk_fence(D);
        SCHED();
        const float e0 = cb1 ? (cb0 ? D[3][3] : D[2][2]) : (cb0 ? D[1][1] : D[0][0]);
        const float e1 = cb1 ? (cb0 ? D[2][3] : D[3][2]) : (cb0 ? D[0][1] : D[1][0]);
        const float e2 = cb1 ? (cb0 ? D[1][3] : D[0][2]) : (cb0 ? D[3][1] : D[2][0]);
        const float e3 = cb1 ? (cb0 ? D[0][3] : D[1][2]) : (cb0 ? D[2][1] : D[3][0]);
        X[hf] = (e0 + dppf<XOR1>(e1)) + (dppf<XOR2>(e2) + dppf<XOR3>(e3));
      });
      float mx = fmaxf(X[0], X[1]);
      if (__any(mx > m)) {
        mx = fmaxf(mx, dppf<XOR1>(mx));
        mx = fmaxf(mx, dppf<XOR2>(mx));
        mx = xrow_max(mx);
        const float mn = fmaxf(m, mx);
        const float mcn = (mn == -INFINITY) ? 0.f : mn * C2;
        const float alpha = __builtin_amdgcn_exp2f(m * C2 - mcn);
        l *= alpha;
        acc[0] *= alpha; acc[1] *= alpha;
        m = mn; mc = mcn;
      }
      const float p0 = __builtin_amdgcn_exp2f(fmaf(X[0], C2, -mc)), p1 = __builtin_amdgcn_exp2f(fmaf(X[1], C2, -mc));
      l += p0 + p1;
      const u32 Y = pack_bf16(p0, p1);
      const v4u Yb = {dppu<0x00>(Y), dppu<0x55>(Y), dppu<0xAA>(Y), dppu<0xFF>(Y)};
#pragma unroll
      for (int es = 0; es < 4; ++es) {
        const bool on = c4 == es;
        const v4u pv = {on ? Yb.x : 0u, on ? Yb.y : 0u, on ? Yb.z : 0u, on ? Yb.w : 0u};
        if (home(T, es) == 0) pv2a(acc[0], acc[1], vresA[hidx(T, es)][es], vresA[hidx(T, es)][4 + es], pv);
        else if (home(T, es) == 2) pv2v(acc[0], acc[1], vresV[hidx(T, es)][es], vresV[hidx(T, es)][4 + es], pv);
        else pv2v(acc[0], acc[1], ldsV[lslot(T, es) * 64], ldsV[lslot(T, 4 + es) * 64], pv);
      }
      SCHED();
    });
    asm volatile("" : "+v"(ln));

    l += dppf<XOR1>(l); l += dppf<XOR2>(l); l = xrow_sum(l);
    asm volatile("s_nop 7" : "+v"(acc[0]), "+v"(acc[1]));
    {
      f32x4* mrg = (f32x4*)(smem + L_MRG);
      mrg[(w * 2 + 0) * 64 + ln] = acc[0];
      mrg[(w * 2 + 1) * 64 + ln] = acc[1];
      f32x2* ml = (f32x2*)(smem + L_ML);
      if (ln < 16 && (ln & 3) == 0) ml[(ln >> 2) * 4 + w] = f32x2{mc, l};
      BAR();
      if (w < 2) {
        const int h = (ln >> 2) & 3;
        const f32x4 qa = *(const f32x4*)(ml + h * 4), qb = *(const f32x4*)(ml + h * 4 + 2);
        const float M = fmaxf(fmaxf(qa.x, qa.z), fmaxf(qb.x, qb.z));
        const float f0 = __builtin_amdgcn_exp2f(qa.x - M), f1 = __builtin_amdgcn_exp2f(qa.z - M);
        const float f2 = __builtin_amdgcn_exp2f(qb.x - M), f3 = __builtin_amdgcn_exp2f(qb.z - M);
        const f32x4 o = (f0 * mrg[w * 64 + ln] + f1 * mrg[(2 + w) * 64 + ln]) + (f2 * mrg[(4 + w) * 64 + ln] + f3 * mrg[(6 + w) * 64 + ln]);
        const float L = fmaf(f0, qa.y, f1 * qa.w) + fmaf(f2, qb.y, f3 * qb.w);
        const int pos = (w * 32 + (ln >> 4) * 8 + (h & 1) * 4 + (ln & 3)) * 16 + (h >> 1) * (32 * PA_REC);
        const int rec = OFF_PA + ((layer * 16 + 2 * kvh) * 32 + sp) * PA_REC;
        const v4u ov = {canon(f2u(o.x)), canon(f2u(o.y)), canon(f2u(o.z)), canon(f2u(o.w))};
        __builtin_amdgcn_raw_buffer_store_b128(ov, rsW, pos, rec, AUX_SC1);
        if (w == 0 && ln < 16 && (ln & 3) == 0)
          __builtin_amdgcn_raw_buffer_store_b64(v2u{canon(f2u(M)), canon(f2u(L))}, rsW, 1024 + (h & 1) * 8 + (h >> 1) * (32 * PA_REC), rec, AUX_SC1);
      }
    }

    {
      if (w >= 2) __builtin_amdgcn_s_sleep(8);
      if (DLY_A) __builtin_amdgcn_s_sleep(DLY_A);
      v4u pa[8];
      v2u mlv;
      const int base = OFF_PA + (layer * 16 + rg) * 32 * PA_REC;
      CLOBBER();
      mlv = __builtin_amdgcn_raw_buffer_load_b64(rsW, 1024 + (ln >> 1) * PA_REC + (ln & 1) * 8, base, AUX_SC1);
#pragma unroll
      for (int k = 0; k < 8; ++k) pa[k] = ld128<AUX_SC1>(rsW, ln * 16, base + (8 * w + k) * PA_REC);
      SCHED();
      int rxo = ((8 * (ln & 3) + 4 * (ln >> 5) + 2 * ((ln >> 4) & 1)) * 4 + w) * 4, ppo = (16 * w + (ln >> 2)) * 16;
      asm volatile("" : "+v"(rxo), "+v"(ppo));
      f32x2 wof[4][4];
#pragma unroll
      for (int r = 0; r < 4; ++r)
#pragma unroll
        for (int i = 0; i < 4; ++i) { if (i == 0) asm volatile("" : "+v"(wot[r])); wof[r][i] = f32x2{bflo(wot[r][i]), bfhi(wot[r][i])}; asm volatile("" : "+v"(wof[r][i])); }
      SCHED();
_Pragma("unroll 1")
      for (int spin = 0; spin < SPIN_CAP; ++spin) {
        u32 mk[8], mxw = umax(mlv.x, mlv.y);
#pragma unroll
        for (int k = 0; k < 8; ++k) { mk[k] = max4(pa[k]); mxw = umax(mxw, mk[k]); }
        if (__all(mxw != SENT)) break;
        u32 need = __any(umax(mlv.x, mlv.y) == SENT) ? 0x100u : 0u;
#pragma unroll
        for (int k = 0; k < 8; ++k) need |= __any(mk[k] == SENT) ? (1u << k) : 0u;
        CLOBBER();
        if (need & 0x100) mlv = __builtin_amdgcn_raw_buffer_load_b64(rsW, 1024 + (ln >> 1) * PA_REC + (ln & 1) * 8, base, AUX_SC1);
#pragma unroll
        for (int k = 0; k < 8; ++k)
          if (need & (1 << k)) pa[k] = ld128<AUX_SC1>(rsW, ln * 16, base + (8 * w + k) * PA_REC);
        SCHED();
      }
      __builtin_amdgcn_s_waitcnt(0x0F70);
      if (sp == 0 && w == 0 && ln == 0) __builtin_amdgcn_raw_buffer_store_b32(1u, rsW, 0, OFF_FL + (layer * 8 + kvh) * 128, AUX_SC1);
      const float mm = u2f(mlv.x);
      float M = fmaxf(mm, dppf<XOR2>(mm));
      M = fmaxf(M, dppf<0x124>(M));
      M = fmaxf(M, dppf<ROR8>(M));
      M = xrow_max(M);
      const float wgt = __builtin_amdgcn_exp2f(mm - M);
      float L = wgt * u2f(mlv.y);
      L += dppf<XOR2>(L);
      L += dppf<0x124>(L);
      L += dppf<ROR8>(L);
      L = xrow_sum(L);
      const float fac = wgt * __builtin_amdgcn_rcpf(L);
      f32x4 o = {0.f, 0.f, 0.f, 0.f};
      const int src = (16 * w + ((ln >> 2) & 1)) * 4;
      float fk[8];
#pragma unroll
      for (int k = 0; k < 8; ++k) fk[k] = u2f((u32)__builtin_amdgcn_ds_bpermute(src + 8 * k, (int)f2u(fac)));
      SCHED();
#pragma unroll
      for (int k = 0; k < 8; ++k) o += fk[k] * __builtin_bit_cast(f32x4, pa[k]);
      f32x4* op = (f32x4*)(smem + L_MRG);
      op[w * 128 + ln] = o;
      BAR();
      const f32x4* opp = (const f32x4*)(smem + L_MRG + ppo);
      o = (opp[0] + opp[128]) + (opp[256] + opp[384]);
      float a[8];
#pragma unroll
      for (int i = 0; i < 4; ++i) {
        const f32x2 t = (o.x * wof[0][i] + o.y * wof[1][i]) + (o.z * wof[2][i] + o.w * wof[3][i]);
        a[2 * i] = t.x; a[2 * i + 1] = t.y;
      }
#pragma unroll
      for (int i = 0; i < 4; ++i) a[i] = swap32_add(a[i], a[i + 4]);
#pragma unroll
      for (int i = 0; i < 2; ++i) {
        a[i] = swap16_add(a[i], a[i + 2]);
        a[i] += dppf<ROR8>(a[i]);
        a[i] += dppf<0x124>(a[i]);
      }
      float* const rx = (float*)(smem + L_RX);
      if ((ln & 12) == 0) {
        float* const rp = (float*)(smem + L_RX + rxo);
        rp[0] = a[0];
        rp[4] = a[1];
      }
      BAR();
      if (w == 0 && ln < 32)
        __builtin_amdgcn_raw_buffer_store_b32(canon(f2u(sum4(*(const f32x4*)(rx + ln * 4)))), rsW, ln * 4, OFF_XB + (layer * 16 + rg) * 2048 + cs * 128, AUX_SC1);
    }

    {
      v4u wt[4];
#pragma unroll
      for (int i = 0; i < 4; ++i) wt[i] = ldsW13[i * 256 + w * 64 + ln];
      f32x2 wf[2][8];
      unpack_w(wt, wf);
      float cs13 = sum4(*(const f32x4*)(red + 5 * RED_STRIDE + (ln & 15) * 4));
      f32x2 xi = ((const f32x2*)(smem + L_XI))[w * 64 + ln];
      const int ro = red_slot(w, ln);
      int own = w * 128 + 4 * (ln & 31) + 2 * (ln >> 5) - dcol;
      asm volatile("" : "+v"(cs13), "+v"(xi), "+v"(own));
_Pragma("unroll 1")
      for (int spin = 0; spin < SPIN_CAP; ++spin) {
        CLOBBER();
        const u32 fl = __builtin_amdgcn_raw_buffer_load_b32(rsW, (ln & 7) * 128, OFF_FL + layer * 1024, AUX_SC1);
        if (__all(fl != SENT || (ln & 7) == kvh)) break;
      }
      v4u g[8];
_Pragma("unroll 1")
      for (int spin = 0; spin < SPIN_CAP; ++spin) {
        CLOBBER();
#pragma unroll
        for (int k = 0; k < 8; ++k) g[k] = ld128<AUX_SC1>(rsW, (ln & 31) * 16 + (ln >> 5) * 2048, OFF_XB + layer * 32768 + w * 512 + k * 4096);
        u32 mxw = 0;
#pragma unroll
        for (int k = 0; k < 8; ++k) mxw = umax(mxw, max4(g[k]));
        if (__all(mxw != SENT)) break;
        __builtin_amdgcn_s_sleep(DLY_RETRY);
      }
      const f32x4 sm = ((__builtin_bit_cast(f32x4, g[0]) + __builtin_bit_cast(f32x4, g[1])) + (__builtin_bit_cast(f32x4, g[2]) + __builtin_bit_cast(f32x4, g[3]))) +
                       ((__builtin_bit_cast(f32x4, g[4]) + __builtin_bit_cast(f32x4, g[5])) + (__builtin_bit_cast(f32x4, g[6]) + __builtin_bit_cast(f32x4, g[7])));
      const float x1a = xi.x + swap32_add(sm.x, sm.z), x1b = xi.y + swap32_add(sm.y, sm.w);
      if (own == 0) *(f32x2*)(red + 3 * RED_STRIDE) = f32x2{x1a, x1b};
      ln_gemv_part(x1a - cmean, x1b - cmean, wf, red + RED_STRIDE, ro, ln);
      BAR();
      float mu;
      const float gt = ln_gemv_final(red + RED_STRIDE, cs13, ln, mu);
      const float up = dppf<ROR8>(gt);
      const float hval = gt * __builtin_amdgcn_rcpf(1.0f + __expf(-gt)) * up;
      if (doS3 && w == 0 && ln < 8) __builtin_amdgcn_raw_buffer_store_b32(canon(f2u(hval)), rsW, ln * 4, OFF_H + layer * 8192 + s3 * 32, AUX_SC1);
      cmean = u2f((u32)__builtin_amdgcn_readfirstlane((int)f2u(cmean + mu)));
    }

    {
      f32x2 w2f[7];
#pragma unroll
      for (int k = 0; k < 7; ++k) {
        u32 t;
        if (k < 4) t = ((const u32*)(smem + L_W2))[k * 256 + w * 64 + ln];
        else { asm volatile("" : "+v"(w2r[k])); t = w2r[k]; }
        w2f[k] = f32x2{bflo(t), bfhi(t)};
        asm volatile("" : "+v"(w2f[k]));
      }
      if constexpr (layer == 0) { SCHED(); static_for<0, NPRE / 2>(issue2); SCHED(); }
      __builtin_amdgcn_s_sleep(DLY_C1);
      u32 hv[7];
_Pragma("unroll 1")
      for (int spin = 0; spin < SPIN_CAP; ++spin) {
        CLOBBER();
        u32 mxw = 0;
#pragma unroll
        for (int k = 0; k < 7; ++k) hv[k] = __builtin_amdgcn_raw_buffer_load_b32(rsW, ln * 4, OFF_H + layer * 8192 + w * 256 + k * 1024, AUX_SC1);
#pragma unroll
        for (int k = 0; k < 7; ++k) mxw = umax(mxw, hv[k]);
        if (__all(mxw != SENT)) break;
        __builtin_amdgcn_s_sleep(DLY_RETRY);
      }
      f32x2 a = u2f(hv[0]) * w2f[0];
#pragma unroll
      for (int k = 1; k < 7; ++k) a += u2f(hv[k]) * w2f[k];
      float r = swap32_add(a.x, a.y);
      r = swap16_add(r, r);
      r += dppf<XOR1>(r); r += dppf<XOR2>(r); r += dppf<HMIR>(r); r += dppf<MIR>(r);
      float* const rd = red + 2 * RED_STRIDE;
      if ((ln & 31) == 0) rd[(ln >> 5) * 4 + w] = r;
      BAR();
      if (w == 0 && ln == 0) {
        const f32x2 x1 = *(const f32x2*)(red + 3 * RED_STRIDE);
        const float y0 = x1.x + sum4(*(const f32x4*)rd), y1 = x1.y + sum4(*(const f32x4*)(rd + 4));
        if (layer == 0) __builtin_amdgcn_raw_buffer_store_b64(v2u{canon(f2u(y0)), canon(f2u(y1))}, rsW, 0, OFF_X2 + dcol * 4, AUX_SC1);
        else *(u32*)(p.out + dcol) = pack_bf16(y0, y1);
      }
    }
    if constexpr (layer == 0) {

    {
      v4u wt[4];
#pragma unroll
      for (int i = 0; i < 4; ++i) wt[i] = ldsWq[i * 256 + w * 64 + ln];
      f32x2 wf[2][8];
      unpack_w(wt, wf);
      float csq = sum4(*(const f32x4*)(red + 4 * RED_STRIDE + (ln & 15) * 4));
      asm volatile("" : "+v"(csq));
      const int ro = red_slot(w, ln);
      SCHED(); static_for<NPRE / 2, NPRE>(issue2); SCHED();
      __builtin_amdgcn_s_sleep(DLY_C2);
      v2u g;
_Pragma("unroll 1")
      for (int spin = 0; spin < SPIN_CAP; ++spin) {
        CLOBBER();
        g = __builtin_amdgcn_raw_buffer_load_b64(rsW, (ln & 31) * 16 + (ln >> 5) * 8, OFF_X2 + w * 512, AUX_SC1);
        if (__all(umax(g.x, g.y) != SENT)) break;
        __builtin_amdgcn_s_sleep(DLY_RETRY);
      }
      const float xia = u2f(g.x), xib = u2f(g.y);
      ((v2u*)(smem + L_XI))[w * 64 + ln] = g;
      ln_gemv_part(xia - cmean, xib - cmean, wf, red, ro, ln);
      BAR();
      float mu;
      const float qv = ln_gemv_final(red, csq, ln, mu);
      const u32 pk = canon(pack_bf16(qv, dppf<XOR1>(qv)));
      if (w == 0 && ln < 16 && (ln & 1) == 0) __builtin_amdgcn_raw_buffer_store_b32(pk, rsW, ln * 2, OFF_Q1 + qcol * 8, AUX_SC1);
      cmean = u2f((u32)__builtin_amdgcn_readfirstlane((int)f2u(cmean + mu)));
    }
    int qo = voffK;
    asm volatile("" : "+v"(qo));
    qo = qoff(qo);
    __builtin_amdgcn_s_sleep(DLY_D);
_Pragma("unroll 1")
    for (int spin = 0; spin < SPIN_CAP; ++spin) {
      CLOBBER();
      qf = ld128<AUX_SC1>(rsW, qo, OFF_Q1 + kvh * 4096);
      if (__all(max4(qf) != SENT)) break;
    }
    }
  };
  layer_body(IC<0>{});
  layer_body(IC<1>{});
  asm volatile(".Ldk_code_end:");
}

extern "C" void kernel_launch(void* const* d_in, const int* in_sizes, int n_in, void* d_out, int out_size,
                              void* d_ws, size_t ws_size, hipStream_t stream) {
  Params p;
  p.x = (const u16*)d_in[0];
  p.K = (const u16*)d_in[1];
  p.V = (const u16*)d_in[2];
  p.wq = (const u16*)d_in[3];
  p.wo = (const u16*)d_in[4];
  p.w1 = (const u16*)d_in[6];
  p.w3 = (const u16*)d_in[7];
  p.w2 = (const u16*)d_in[8];
  p.out = (u16*)d_out;
  p.ws = (unsigned char*)d_ws;
  decode_kernel<<<256, 256, 0, stream>>>(p);
}
```

```cpp
#include <hip/hip_runtime.h>
#include <stdint.h>

typedef unsigned u32;
typedef unsigned long long u64;
typedef unsigned short u16;
typedef u32 v4u __attribute__((ext_vector_type(4)));
typedef u32 v2u __attribute__((ext_vector_type(2)));
typedef float f32x4 __attribute__((ext_vector_type(4)));
typedef float f32x2 __attribute__((ext_vector_type(2)));
typedef __bf16 bf16x2 __attribute__((ext_vector_type(2)));
typedef __amdgpu_buffer_rsrc_t rsrc_t;

constexpr int AUX_NT = 2;
constexpr int AUX_SC1 = 16;
constexpr u32 SENT = 0xFFFFFFFFu;
constexpr int SPIN_CAP = 200000;

constexpr int OFF_EPOCH = 0;
constexpr int OFF_READY = 4096;
constexpr int OFF_Q0 = 8192;
constexpr int OFF_HO = OFF_Q0 + 32768;
constexpr int PA_REC = 1152;
constexpr int OFF_PA = OFF_HO;
constexpr int OFF_XB = OFF_PA + 2 * 16 * 32 * PA_REC;
constexpr int OFF_H = OFF_XB + 2 * 16 * 2048;
constexpr int OFF_X2 = OFF_H + 2 * 8192;
constexpr int OFF_FL = OFF_X2 + 2048;
constexpr int OFF_Q1 = OFF_FL + 2048;
constexpr int HO_USED = OFF_Q1 + 32768 - OFF_HO;
constexpr int HO_SLICE = 5120;
static_assert(HO_USED <= 256 * HO_SLICE, "reset slices must cover the hand-off region");
constexpr int OFF_STAMP = 4 << 20;
constexpr int WS_BYTES = OFF_HO + 256 * HO_SLICE;

constexpr int RED_STRIDE = 80;
constexpr int L_RED = 0;
constexpr int L_ML = L_RED + 6 * RED_STRIDE * 4;
constexpr int L_MRG = L_ML + 128;
constexpr int L_RX = L_MRG + 1024;
constexpr int L_XI = L_MRG + 8192;
constexpr int L_W2 = L_XI + 2048;
constexpr int L_WQ = L_W2 + 4096;
constexpr int L_W13 = L_WQ + 16384;
constexpr int L_V = L_W13 + 16384;
constexpr int L_BYTES = L_V + 114688;
static_assert(L_BYTES <= 163840 && L_V % 16 == 0, "LDS");

#define DLY_A 16
#define DLY_C1 12
#define DLY_C2 12
#define DLY_D 5
#define DLY_RETRY 3

struct Params {
  const u16* x; const u16* K; const u16* V; const u16* wq; const u16* wo;
  const u16* w1; const u16* w3; const u16* w2; u16* out; unsigned char* ws;
};

__device__ __forceinline__ float u2f(u32 x) { return __builtin_bit_cast(float, x); }
__device__ __forceinline__ u32 f2u(float x) { return __builtin_bit_cast(u32, x); }
__device__ __forceinline__ float bflo(u32 x) { return u2f(x << 16); }
__device__ __forceinline__ float bfhi(u32 x) { return u2f(x & 0xffff0000u); }
__device__ __forceinline__ u32 pack_bf16(float lo, float hi) {
  bf16x2 r = {(__bf16)lo, (__bf16)hi};
  return __builtin_bit_cast(u32, r);
}
__device__ __forceinline__ u32 canon(u32 x) { return x < SENT - 1u ? x : SENT - 1u; }
template <int CTRL> __device__ __forceinline__ float dppf(float x) {
  return u2f((u32)__builtin_amdgcn_mov_dpp((int)f2u(x), CTRL, 0xf, 0xf, true));
}
template <int CTRL> __device__ __forceinline__ u32 dppu(u32 x) {
  return (u32)__builtin_amdgcn_mov_dpp((int)x, CTRL, 0xf, 0xf, true);
}
constexpr int XOR1 = 0xB1, XOR2 = 0x4E, XOR3 = 0x1B, HMIR = 0x141, MIR = 0x140, ROR8 = 0x128;
__device__ __forceinline__ float swap32_add(float a, float b) {
  auto r = __builtin_amdgcn_permlane32_swap(f2u(a), f2u(b), false, false);
  return u2f(r[0]) + u2f(r[1]);
}
__device__ __forceinline__ float swap16_add(float a, float b) {
  auto r = __builtin_amdgcn_permlane16_swap(f2u(a), f2u(b), false, false);
  return u2f(r[0]) + u2f(r[1]);
}
__device__ __forceinline__ float xrow_max(float x) {
  auto s = __builtin_amdgcn_permlane16_swap(f2u(x), f2u(x), false, false);
  x = fmaxf(u2f(s[0]), u2f(s[1]));
  auto t = __builtin_amdgcn_permlane32_swap(f2u(x), f2u(x), false, false);
  return fmaxf(u2f(t[0]), u2f(t[1]));
}
__device__ __forceinline__ float xrow_sum(float x) {
  x = swap16_add(x, x);
  return swap32_add(x, x);
}
__device__ __forceinline__ float treduce16(float (&v)[16], int lane) {
#pragma unroll
  for (int i = 0; i < 8; ++i) v[i] = swap32_add(v[i], v[i + 8]);
#pragma unroll
  for (int i = 0; i < 4; ++i) v[i] = swap16_add(v[i], v[i + 4]);
  const bool b3 = lane & 8, b2 = lane & 4;
#pragma unroll
  for (int i = 0; i < 2; ++i) {
    const float keep = b3 ? v[i + 2] : v[i], send = b3 ? v[i] : v[i + 2];
    v[i] = keep + dppf<ROR8>(send);
  }
  const float keep = b2 ? v[1] : v[0], send = b2 ? v[0] : v[1];
  float r = keep + dppf<HMIR>(send);
  r += dppf<XOR2>(r);
  r += dppf<XOR1>(r);
  return r;
}
__device__ __forceinline__ f32x4 qk1(v4u k, v4u q) {
  f32x4 d;
  asm volatile("v_mfma_f32_16x16x32_bf16 %0, %1, %2, 0" : "=&v"(d) : "v"(k), "v"(q));
  return d;
}
__device__ __forceinline__ void qk_fence(f32x4 (&D)[4]) {
  asm volatile("s_nop 7" : "+v"(D[0]), "+v"(D[1]), "+v"(D[2]), "+v"(D[3]));
}
__device__ __forceinline__ void pv2a(f32x4& a0, f32x4& a1, v4u va, v4u vb, v4u pv) {
  asm volatile("s_nop 1\n\t"
      "v_mfma_f32_16x16x32_bf16 %0, %2, %4, %0\n\t"
      "v_mfma_f32_16x16x32_bf16 %1, %3, %4, %1"
      : "+v"(a0), "+v"(a1) : "a"(va), "a"(vb), "v"(pv));
}
__device__ __forceinline__ void pv2v(f32x4& a0, f32x4& a1, v4u va, v4u vb, v4u pv) {
  asm volatile("s_nop 1\n\t"
      "v_mfma_f32_16x16x32_bf16 %0, %2, %4, %0\n\t"
      "v_mfma_f32_16x16x32_bf16 %1, %3, %4, %1"
      : "+v"(a0), "+v"(a1) : "v"(va), "v"(vb), "v"(pv));
}
__device__ __forceinline__ v4u ldw(const void* base, u32 off) {
  return *(const v4u*)((const unsigned char*)base + off);
}
constexpr int home(int T, int es) { return T >= 12 ? 2 : T == 11 ? (es < 2 ? 1 : 2) : (T % 3 == 2 ? 1 : 0); }
constexpr int hidx(int T, int es) { return T >= 12 ? T - 12 : T == 11 ? (es < 2 ? 3 : 4) : (T % 3 == 2 ? T / 3 : T - T / 3); }
constexpr int lslot(int T, int e) { return T == 11 ? 24 + (e >> 2) * 2 + (e & 3) : (T / 3) * 8 + e; }
template <int AUX> __device__ __forceinline__ v4u ld128(rsrc_t rs, int voff, int soff) {
  return __builtin_amdgcn_raw_buffer_load_b128(rs, voff, soff, AUX);
}
__device__ __forceinline__ int chunk(int j, int rot4k) { return (j * 4096 + rot4k) & 0x7FFFF; }
#define RD 3
#define TLBW 1
#define MALLM 3
#define MALLN 1
constexpr int NPRE = 8;
constexpr int order2(int st) { return st == 0 ? 12 : st < 7 ? ((st & 1) ? st / 2 : 12 + st / 2) : st == 14 ? 11 : st == 15 ? 10 : st - 4; }
constexpr int blk2(int i) { return 8 * order2(i >> 3) + (i & 7); }
template <bool FIRST> __device__ __forceinline__ v4u ldk(rsrc_t rs, int voff, int j, int rot4k) {
  if (FIRST && (j >= 96 || j % MALLM < MALLN)) return ld128<0>(rs, voff, chunk(j, rot4k));
  return ld128<AUX_NT>(rs, voff, chunk(j, rot4k));
}
constexpr int W1 = 18;
constexpr int win2(int i) { return i < 8 ? 10 : i < 24 ? 18 : i < 48 ? 20 : 12; }
constexpr int cmin(int a, int b) { return a < b ? a : b; }
constexpr int cmax(int a, int b) { return a > b ? a : b; }
constexpr int hi_start(int st);
constexpr int hi_after(int i) { return i < 0 ? NPRE : cmax((i & 7) ? hi_after(i - 1) : hi_start(i >> 3), cmin(128, i + 1 + win2(i))); }
constexpr int hi_start(int st) { return cmax(hi_after(8 * st - 1), cmin(128, 8 * st + win2(8 * st))); }
__device__ __forceinline__ int qoff(int voffK) { return ((voffK >> 5) & 31) * 128 + (voffK & 31); }
__device__ __forceinline__ u32 umax(u32 a, u32 b) { return a > b ? a : b; }
__device__ __forceinline__ u32 max4(v4u v) { return umax(umax(v.x, v.y), umax(v.z, v.w)); }
#define BAR() asm volatile("s_waitcnt lgkmcnt(0)\n\ts_barrier" ::: "memory")
#define CLOBBER() asm volatile("" ::: "memory")
#define SCHED() __builtin_amdgcn_sched_barrier(0)
#define STAMP(i) do { } while (0)

__device__ __forceinline__ void unpack_w(const v4u (&wt)[4], f32x2 (&wf)[2][8]) {
#pragma unroll
  for (int r = 0; r < 2; ++r)
#pragma unroll
    for (int i = 0; i < 4; ++i) {
      wf[r][i] = f32x2{bflo(wt[r][i]), bfhi(wt[r][i])};
      wf[r][4 + i] = f32x2{bflo(wt[2 + r][i]), bfhi(wt[2 + r][i])};
    }
#pragma unroll
  for (int r = 0; r < 2; ++r)
#pragma unroll
    for (int i = 0; i < 8; ++i) asm volatile("" : "+v"(wf[r][i]));
}
__device__ __forceinline__ int red_slot(int w, int lane) {
  int o = (lane & 3) == 0 ? (lane + w) * 4 : (lane & 31) == 1 ? ((16 + (lane >> 5)) * 4 + w) * 4 : -1;
  asm volatile("" : "+v"(o));
  return o;
}
__device__ __forceinline__ void ln_gemv_part(float da, float db, const f32x2 (&wf)[2][8], float* red, int ro, int lane) {
  float a[16];
#pragma unroll
  for (int i = 0; i < 8; ++i) {
    const f32x2 t = da * wf[0][i] + db * wf[1][i];
    a[2 * i] = t.x; a[2 * i + 1] = t.y;
  }
  float st = swap32_add(da + db, fmaf(da, da, db * db));
  st = swap16_add(st, st);
  st += dppf<XOR1>(st); st += dppf<XOR2>(st); st += dppf<HMIR>(st); st += dppf<MIR>(st);
  const float r = treduce16(a, lane);
  float v = (lane & 1) ? st : r;
  asm volatile("" : "+v"(v));
  if (ro >= 0) *(float*)((unsigned char*)red + ro) = v;
}
__device__ __forceinline__ float sum4(f32x4 v) { return (v.x + v.y) + (v.z + v.w); }
__device__ __forceinline__ float ln_gemv_final(const float* red, float cs, int lane, float& mu) {
  const f32x4 g = *(const f32x4*)(red + (lane & 15) * 4), s1 = *(const f32x4*)(red + 64), s2 = *(const f32x4*)(red + 68);
  mu = sum4(s1) * (1.0f / 512.0f);
  const float var = fmaf(-mu, mu, sum4(s2) * (1.0f / 512.0f));
  return fmaf(-mu, cs, sum4(g)) * __builtin_amdgcn_rsqf(fmaxf(var, 0.f) + 1e-5f);
}

template <int I> struct IC { static constexpr int value = I; };
template <int I, int N, class F> __device__ __forceinline__ void static_for(F&& f) {
  if constexpr (I < N) { f(IC<I>{}); static_for<I + 1, N>(f); }
}

__global__ void __launch_bounds__(256, 1) decode_kernel(Params p) {
  __shared__ __attribute__((aligned(16))) unsigned char smem[L_BYTES];
  const int tid = threadIdx.x, lane = tid & 63, w = __builtin_amdgcn_readfirstlane(tid >> 6);
  const int b = blockIdx.x, kvh = b & 7, sp = b >> 3;
  const rsrc_t rsW = __builtin_amdgcn_make_buffer_rsrc((void*)p.ws, 0, WS_BYTES, 0x00020000);
  const size_t kvoff = ((size_t)kvh * 65536 + (size_t)sp * 2048) * 128;
  const rsrc_t rsK = __builtin_amdgcn_make_buffer_rsrc((void*)(p.K + kvoff), 0, 2048 * 256, 0x00020000);
  const rsrc_t rsV = __builtin_amdgcn_make_buffer_rsrc((void*)(p.V + kvoff), 0, 2048 * 256, 0x00020000);

  float* const red = (float*)(smem + L_RED);
  u64 code0;
  u32 code_len;
  asm volatile("s_getpc_b64 %0\n.Ldk_code_begin:\n\ts_mov_b32 %1, .Ldk_code_end-.Ldk_code_begin" : "=s"(code0), "=s"(code_len));
  const rsrc_t rsC = __builtin_amdgcn_make_buffer_rsrc((void*)code0, 0, code_len, 0x00020000);

  const int rot4k = ((sp * 5 + kvh * 16) & 127) * 4096;
  int rotK1 = rot4k, rotK2 = rot4k;
  asm volatile("" : "+s"(rotK1));
  asm volatile("" : "+s"(rotK2));
  const int rgw = 2 * kvh + (sp & 1);
  const int voffV = lane * 16 + w * 1024;
  const int voffK = ((lane >> 2) & 3) * 256 + ((lane >> 4) * 4 + (lane & 3)) * 16 + w * 1024;
  v4u vresA[8][8], vresV[5][8];
  v4u raw[RD][8], rawL[2][8];
  v4u k1[128], k2[128];
#pragma unroll
  for (int T = 0; T < RD; ++T)
#pragma unroll
    for (int t = 0; t < 8; ++t) raw[T][t] = ld128<AUX_NT>(rsV, voffV, chunk(8 * T + t, rot4k));
  SCHED();
  typedef const __attribute__((address_space(4))) u32* cptr;
  u32 warm[15];
  if (TLBW) {
    warm[0] = *(cptr)((const unsigned char*)p.wq + 0); warm[1] = *(cptr)((const unsigned char*)p.wq + (2 << 20) + 64); warm[2] = *(cptr)((const unsigned char*)p.wq + (4 << 20) - 64);
    warm[3] = *(cptr)((const unsigned char*)p.w1 + 0); warm[4] = *(cptr)((const unsigned char*)p.w1 + 512 * 3584 - 64);
    warm[5] = *(cptr)((const unsigned char*)p.w3 + 0); warm[6] = *(cptr)((const unsigned char*)p.w3 + 512 * 3584 - 64);
    warm[7] = *(cptr)((const unsigned char*)p.w2 + 0); warm[8] = *(cptr)((const unsigned char*)p.w2 + 1792 * 1024 - 64);
    warm[9] = *(cptr)((const unsigned char*)p.wo + rgw * 262144);
    warm[10] = *(cptr)(p.K + kvoff);
    warm[11] = *(cptr)p.x;
    warm[12] = *(cptr)(p.ws + 2048); warm[13] = *(cptr)(p.ws + WS_BYTES + 4096); warm[14] = *(cptr)p.out;
    SCHED();
  }
  u32 touch[6];
  touch[0] = *(const u32*)((const unsigned char*)p.wq + (u32)((16 * sp + 4 * w + (lane >> 4)) * 8192 + kvh * 1024 + (lane & 15) * 64));
  touch[3] = __builtin_amdgcn_raw_buffer_load_b32(rsC, tid * 64, sp * 16384, 0);
  if (w == 0) {
    const v4u s4 = {SENT, SENT, SENT, SENT};
#pragma unroll
    for (int i = 0; i < 5; ++i) __builtin_amdgcn_raw_buffer_store_b128(s4, rsW, OFF_HO + lane * 16 + i * 1024, b * HO_SLICE, AUX_SC1);
  }
  const v2u ep = __builtin_amdgcn_raw_buffer_load_b64(rsW, OFF_EPOCH, 0, AUX_SC1);
  const int row0 = w * 128 + 4 * (lane & 31) + 2 * (lane >> 5);
  const int qcol = kvh * 512 + sp * 16;
  const int rg = 2 * kvh + (sp & 1), cs = sp >> 1;
  const bool doS3 = sp < 28;
  const int s3 = kvh * 28 + (doS3 ? sp : 0);
  const int dcol = kvh * 64 + sp * 2;
  u32 x01 = 0, x00 = 0;
  SCHED();

  float cmean = 0.f;
  u32 tag_lo = 0, tag_hi = 0;
  u32 w2r[7];
  v4u qf = {0u, 0u, 0u, 0u}, r0 = qf, r1 = qf;
  v4u wtmp[8], wot[4];
  v4u* const ldsV = (v4u*)(smem + L_V) + w * (28 * 64) + lane;
  v4u* const ldsWq = (v4u*)(smem + L_WQ);
  v4u* const ldsW13 = (v4u*)(smem + L_W13);
  auto ingest = [&](auto TC, v4u (&rw)[8], auto&& between) __attribute__((always_inline)) {
    constexpr int T = decltype(TC)::value;
    v4u f[8];
#pragma unroll
    for (int e = 0; e < 8; ++e)
#pragma unroll
      for (int j = 0; j < 4; ++j)
        f[e][j] = __builtin_amdgcn_perm(rw[j + 4][e >> 1], rw[j][e >> 1], (e & 1) ? 0x07060302u : 0x05040100u);
#pragma unroll
    for (int e = 0; e < 8; ++e)
      if (home(T, e & 3) == 2) asm volatile("" : "+v"(f[e]));
    SCHED();
    between();
#pragma unroll
    for (int e = 0; e < 8; ++e) {
      if (home(T, e & 3) == 1) ldsV[lslot(T, e) * 64] = f[e];
      else if (home(T, e & 3) == 2) vresV[hidx(T, e & 3)][e] = f[e];
      else { asm volatile("" : "+a"(f[e])); vresA[hidx(T, e & 3)][e] = f[e]; }
    }
    SCHED();
  };
  auto issue1 = [&](auto UC) __attribute__((always_inline)) {
    constexpr int u = decltype(UC)::value;
    if constexpr (u < 112) k1[u] = ldk<true>(rsK, voffK, u, rotK1);
    else if constexpr (u < 120) rawL[0][u - 112] = ld128<AUX_NT>(rsV, voffV, chunk(u, rot4k));
    else if constexpr (u < 128) k1[u - 8] = ldk<true>(rsK, voffK, u - 8, rotK1);
    else if constexpr (u < 136) rawL[1][u - 128] = ld128<AUX_NT>(rsV, voffV, chunk(u - 8, rot4k));
    else if constexpr (u < 144) k1[u - 16] = ldk<true>(rsK, voffK, u - 16, rotK1);
  };
  static_for<0, 14>([&](auto TC) __attribute__((always_inline)) {
    constexpr int T = decltype(TC)::value;
    ingest(TC, raw[T % RD], [&]() __attribute__((always_inline)) {
    __builtin_amdgcn_s_barrier();
    SCHED();
    if constexpr (T + RD < 14) {
#pragma unroll
      for (int t = 0; t < 8; ++t) raw[T % RD][t] = ld128<AUX_NT>(rsV, voffV, chunk(8 * (T + RD) + t, rot4k));
    } else {
      static_for<(T - 14 + RD) * W1 / RD, (T - 13 + RD) * W1 / RD>(issue1);
    }
    if constexpr (T == 0) {
      if (TLBW) asm volatile("" ::"s"(warm[0]), "s"(warm[1]), "s"(warm[2]), "s"(warm[3]), "s"(warm[4]), "s"(warm[5]), "s"(warm[6]), "s"(warm[7]), "s"(warm[8]), "s"(warm[9]), "s"(warm[10]), "s"(warm[11]), "s"(warm[12]), "s"(warm[13]), "s"(warm[14]));
      const int l7 = lane < 56 ? lane : 55;
      touch[1] = *(const u32*)((const unsigned char*)(w < 2 ? p.w1 : p.w3) + (u32)((16 * sp + 8 * (w & 1) + l7 / 7) * 3584 + kvh * 448 + (l7 % 7) * 64));
    }
    if constexpr (T == 1) {
      x01 = *(const u32*)(p.x + row0);
      x00 = *(const u32*)p.x;
      const int l2 = lane < 28 ? lane : 27;
      touch[2] = *(const u32*)((const unsigned char*)p.w2 + (u32)((56 * sp + 14 * w + (l2 >> 1)) * 1024 + kvh * 128 + (l2 & 1) * 64));
    }
    if constexpr (T == 1 || T == 2) {
#pragma unroll
      for (int i = 2 * (T - 1); i < 2 * (T - 1) + 2; ++i) wtmp[i] = ldw(p.wq, (w * 128 + 32 * i + (lane >> 1)) * 8192 + qcol * 2 + (lane & 1) * 16);
    }
    if constexpr (T >= 3 && T < 7) wtmp[T + 1] = ldw(T < 5 ? p.w1 : p.w3, (row0 + ((T + 1) & 1)) * 3584 + s3 * 16);
    if constexpr (T >= 5 && T < 12) w2r[T - 5] = *(const u32*)((const unsigned char*)p.w2 + (u32)((tid + 256 * (T - 5)) * 1024 + dcol * 2));
    if constexpr (T >= 7 && T < 11) {
      const int pp = 16 * w + (lane >> 2);
      const int orow = rg * 256 + ((pp >> 2) & 1) * 128 + ((pp >> 3) & 3) * 32 + (pp >> 5) * 4 + (pp & 3);
      wot[T - 7] = ldw(p.wo, (orow + 8 * (T - 7)) * 1024 + cs * 64 + (lane & 3) * 16);
    }
    if constexpr (T == 5) touch[4] = __builtin_amdgcn_raw_buffer_load_b32(rsK, (lane & 1) * 0x7FF80, 0, AUX_NT);
    if constexpr (T == 9) touch[5] = *(const u32*)(p.out + 2 * lane);
    if constexpr (T >= 8 && T < 12) ((u32*)(smem + L_W2))[(T - 8) * 256 + tid] = w2r[T - 8];
    if constexpr (T == 5) asm volatile("" ::"v"(touch[0]), "v"(touch[1]), "v"(touch[2]), "v"(touch[3]));
    if constexpr (T == 13) asm volatile("" ::"v"(touch[4]), "v"(touch[5]));
    });
    if constexpr (T == 0) STAMP(24);
    if constexpr (T == 5) {
#pragma unroll
      for (int i = 0; i < 4; ++i) {
        const int rho = 32 * i + (lane >> 1);
        ldsWq[((lane & 1) * 2 + (rho & 1)) * 256 + w * 64 + (rho >> 2) + 32 * ((rho >> 1) & 1)] = wtmp[i];
      }
      v4u wt[4];
#pragma unroll
      for (int i = 0; i < 4; ++i) wt[i] = ldsWq[i * 256 + tid];
      f32x2 wf[2][8];
      unpack_w(wt, wf);
      const float xia = bflo(x01), xib = bfhi(x01);
      ((f32x2*)(smem + L_XI))[tid] = f32x2{xia, xib};
      cmean = bflo(x00);
      const int ro = red_slot(w, lane);
      ln_gemv_part(1.0f, 1.0f, wf, red + 4 * RED_STRIDE, ro, lane);
      ln_gemv_part(xia - cmean, xib - cmean, wf, red, ro, lane);
      BAR();
      const float csq = sum4(*(const f32x4*)(red + 4 * RED_STRIDE + (lane & 15) * 4));
      float mu;
      const float qv = ln_gemv_final(red, csq, lane, mu);
      cmean = u2f((u32)__builtin_amdgcn_readfirstlane((int)f2u(cmean + mu)));
      const u32 pk = canon(pack_bf16(qv, dppf<XOR1>(qv)));
      if (w == 0 && lane < 16 && (lane & 1) == 0) __builtin_amdgcn_raw_buffer_store_b32(pk, rsW, lane * 2, OFF_Q0 + qcol * 8, AUX_SC1);
      const u64 epoch = (((u64)ep.y << 32) | ep.x) + 0x9E3779B97F4A7C15ull;
      tag_lo = (u32)__builtin_amdgcn_readfirstlane((int)(u32)epoch);
      tag_hi = (u32)__builtin_amdgcn_readfirstlane((int)(u32)(epoch >> 32));
      STAMP(1);
      SCHED();
    }
    if constexpr (T == 7) if (w == 0) {
      asm volatile("s_waitcnt vmcnt(16)" ::: "memory");
      if (lane == 0) __builtin_amdgcn_raw_buffer_store_b64(v2u{tag_lo, tag_hi}, rsW, 0, OFF_READY + b * 8, AUX_SC1);
      SCHED();
    }
    if constexpr (T == 9) {
      const v4u wt[4] = {wtmp[4], wtmp[5], wtmp[6], wtmp[7]};
#pragma unroll
      for (int i = 0; i < 4; ++i) ldsW13[i * 256 + tid] = wt[i];
      f32x2 wf[2][8];
      unpack_w(wt, wf);
      ln_gemv_part(1.0f, 1.0f, wf, red + 5 * RED_STRIDE, red_slot(w, lane), lane);
      SCHED();
    }
    if constexpr (T == 11) {
      r0 = ld128<AUX_SC1>(rsW, OFF_READY + lane * 16, 0);
      r1 = ld128<AUX_SC1>(rsW, OFF_READY + 1024 + lane * 16, 0);
      SCHED();
    }
    if constexpr (T == 13) {
      auto not_ready = [&](v4u a, v4u c) {
        const u32 bad = ((a.x ^ tag_lo) | (a.y ^ tag_hi) | (a.z ^ tag_lo) | (a.w ^ tag_hi)) |
                        ((c.x ^ tag_lo) | (c.y ^ tag_hi) | (c.z ^ tag_lo) | (c.w ^ tag_hi));
        return __any(bad != 0);
      };
      if (__builtin_expect(not_ready(r0, r1), 0)) {
_Pragma("unroll 1")
        for (int spin = 0; spin < SPIN_CAP; ++spin) {
          __builtin_amdgcn_s_sleep(8);
          CLOBBER();
          const v4u a = ld128<AUX_SC1>(rsW, OFF_READY + lane * 16, 0), c = ld128<AUX_SC1>(rsW, OFF_READY + 1024 + lane * 16, 0);
          if (!not_ready(a, c)) break;
        }
      }
      CLOBBER();
      qf = ld128<AUX_SC1>(rsW, qoff(voffK), OFF_Q0 + kvh * 4096);
      if (b == 0 && tid == 0) __builtin_amdgcn_raw_buffer_store_b64(v2u{tag_lo, tag_hi}, rsW, OFF_EPOCH, 0, AUX_SC1);
      SCHED();
    }
  });
  STAMP(2);

  const int c4 = lane & 3;
  const bool cb0 = lane & 1, cb1 = lane & 2;
  constexpr float C2 = 0.08838834764831845f * 1.4426950408889634f;

  auto issue2 = [&](auto IC_) __attribute__((always_inline)) {
    constexpr int i = decltype(IC_)::value;
    k2[blk2(i)] = ldk<false>(rsK, voffK, blk2(i), rotK2);
  };
  auto layer_body = [&](auto LC) __attribute__((always_inline)) {
    constexpr int layer = decltype(LC)::value;
    STAMP(3 + 12 * layer);
    float m = -INFINITY, mc = 0.f, l = 0.f;
    f32x4 acc[2] = {f32x4{0.f, 0.f, 0.f, 0.f}, f32x4{0.f, 0.f, 0.f, 0.f}};
    u32 junk = 0u;
    int ln = lane;
    asm volatile("s_nop 1" : "+v"(qf));
    static_for<0, 16>([&](auto SC) __attribute__((always_inline)) {
      constexpr int st = decltype(SC)::value;
      constexpr int T = layer ? order2(st) : st;
      if constexpr (st > 0 && st < 15) { __builtin_amdgcn_s_barrier(); SCHED(); }
      if constexpr (layer == 1) { static_for<hi_after(8 * st - 1), hi_start(st)>(issue2); SCHED(); }
      if constexpr (layer == 0 && T >= 14)
        ingest(IC<T>{}, rawL[T - 14], [&]() __attribute__((always_inline)) { static_for<112 + 16 * (T - 14) + W1, 120 + 16 * (T - 14) + W1>(issue1); });
      if constexpr (layer == 0 && st == 13) { junk = __builtin_amdgcn_raw_buffer_load_b32(rsC, tid * 64, sp * 16384, AUX_SC1); SCHED(); }
      if constexpr (layer == 0 && st == 15) { asm volatile("" ::"v"(junk)); SCHED(); }
      float X[2];
      static_for<0, 2>([&](auto HC) __attribute__((always_inline)) {
        constexpr int hf = decltype(HC)::value;
        f32x4 D[4];
        static_for<0, 4>([&](auto QC) __attribute__((always_inline)) {
          constexpr int t = decltype(QC)::value, blk = 8 * T + 4 * hf + t, i = 8 * st + 4 * hf + t;
          D[t] = qk1(layer ? k2[blk] : k1[blk], qf);
          if constexpr (layer == 0) issue1(IC<(blk < 112 ? blk : blk < 120 ? blk + 8 : blk + 16) + W1>{});
          else static_for<((i & 7) ? hi_after(i - 1) : hi_start(st)), hi_after(i)>(issue2);
          SCHED();
        });
        qk_fence(D);
        SCHED();
        const float e0 = cb1 ? (cb0 ? D[3][3] : D[2][2]) : (cb0 ? D[1][1] : D[0][0]);
        const float e1 = cb1 ? (cb0 ? D[2][3] : D[3][2]) : (cb0 ? D[0][1] : D[1][0]);
        const float e2 = cb1 ? (cb0 ? D[1][3] : D[0][2]) : (cb0 ? D[3][1] : D[2][0]);
        const float e3 = cb1 ? (cb0 ? D[0][3] : D[1][2]) : (cb0 ? D[2][1] : D[3][0]);
        X[hf] = (e0 + dppf<XOR1>(e1)) + (dppf<XOR2>(e2) + dppf<XOR3>(e3));
      });
      float mx = fmaxf(X[0], X[1]);
      if (__any(mx > m)) {
        mx = fmaxf(mx, dppf<XOR1>(mx));
        mx = fmaxf(mx, dppf<XOR2>(mx));
        mx = xrow_max(mx);
        const float mn = fmaxf(m, mx);
        const float mcn = (mn == -INFINITY) ? 0.f : mn * C2;
        const float alpha = __builtin_amdgcn_exp2f(m * C2 - mcn);
        l *= alpha;
        acc[0] *= alpha; acc[1] *= alpha;
        m = mn; mc = mcn;
      }
      const float p0 = __builtin_amdgcn_exp2f(fmaf(X[0], C2, -mc)), p1 = __builtin_amdgcn_exp2f(fmaf(X[1], C2, -mc));
      l += p0 + p1;
      const u32 Y = pack_bf16(p0, p1);
      const v4u Yb = {dppu<0x00>(Y), dppu<0x55>(Y), dppu<0xAA>(Y), dppu<0xFF>(Y)};
#pragma unroll
      for (int es = 0; es < 4; ++es) {
        const bool on = c4 == es;
        const v4u pv = {on ? Yb.x : 0u, on ? Yb.y : 0u, on ? Yb.z : 0u, on ? Yb.w : 0u};
        if (home(T, es) == 0) pv2a(acc[0], acc[1], vresA[hidx(T, es)][es], vresA[hidx(T, es)][4 + es], pv);
        else if (home(T, es) == 2) pv2v(acc[0], acc[1], vresV[hidx(T, es)][es], vresV[hidx(T, es)][4 + es], pv);
        else pv2v(acc[0], acc[1], ldsV[lslot(T, es) * 64], ldsV[lslot(T, 4 + es) * 64], pv);
      }
      SCHED();
    });
    STAMP(4 + 12 * layer);
    asm volatile("" : "+v"(ln));

    l += dppf<XOR1>(l); l += dppf<XOR2>(l); l = xrow_sum(l);
    asm volatile("s_nop 7" : "+v"(acc[0]), "+v"(acc[1]));
    {
      f32x4* mrg = (f32x4*)(smem + L_MRG);
      mrg[(w * 2 + 0) * 64 + ln] = acc[0];
      mrg[(w * 2 + 1) * 64 + ln] = acc[1];
      f32x2* ml = (f32x2*)(smem + L_ML);
      if (ln < 16 && (ln & 3) == 0) ml[(ln >> 2) * 4 + w] = f32x2{mc, l};
      BAR();
      if (w < 2) {
        const int h = (ln >> 2) & 3;
        const f32x4 qa = *(const f32x4*)(ml + h * 4), qb = *(const f32x4*)(ml + h * 4 + 2);
        const float M = fmaxf(fmaxf(qa.x, qa.z), fmaxf(qb.x, qb.z));
        const float f0 = __builtin_amdgcn_exp2f(qa.x - M), f1 = __builtin_amdgcn_exp2f(qa.z - M);
        const float f2 = __builtin_amdgcn_exp2f(qb.x - M), f3 = __builtin_amdgcn_exp2f(qb.z - M);
        const f32x4 o = (f0 * mrg[w * 64 + ln] + f1 * mrg[(2 + w) * 64 + ln]) + (f2 * mrg[(4 + w) * 64 + ln] + f3 * mrg[(6 + w) * 64 + ln]);
        const float L = fmaf(f0, qa.y, f1 * qa.w) + fmaf(f2, qb.y, f3 * qb.w);
        const int pos = (w * 32 + (ln >> 4) * 8 + (h & 1) * 4 + (ln & 3)) * 16 + (h >> 1) * (32 * PA_REC);
        const int rec = OFF_PA + ((layer * 16 + 2 * kvh) * 32 + sp) * PA_REC;
        const v4u ov = {canon(f2u(o.x)), canon(f2u(o.y)), canon(f2u(o.z)), canon(f2u(o.w))};
        __builtin_amdgcn_raw_buffer_store_b128(ov, rsW, pos, rec, AUX_SC1);
        if (w == 0 && ln < 16 && (ln & 3) == 0)
          __builtin_amdgcn_raw_buffer_store_b64(v2u{canon(f2u(M)), canon(f2u(L))}, rsW, 1024 + (h & 1) * 8 + (h >> 1) * (32 * PA_REC), rec, AUX_SC1);
      }
    }
    STAMP(5 + 12 * layer);

    {
      if (w >= 2) __builtin_amdgcn_s_sleep(8);
      if (DLY_A) __builtin_amdgcn_s_sleep(DLY_A);
      v4u pa[8];
      v2u mlv;
      const int base = OFF_PA + (layer * 16 + rg) * 32 * PA_REC;
      CLOBBER();
      mlv = __builtin_amdgcn_raw_buffer_load_b64(rsW, 1024 + (ln >> 1) * PA_REC + (ln & 1) * 8, base, AUX_SC1);
#pragma unroll
      for (int k = 0; k < 8; ++k) pa[k] = ld128<AUX_SC1>(rsW, ln * 16, base + (8 * w + k) * PA_REC);
      SCHED();
      int rxo = ((8 * (ln & 3) + 4 * (ln >> 5) + 2 * ((ln >> 4) & 1)) * 4 + w) * 4, ppo = (16 * w + (ln >> 2)) * 16;
      asm volatile("" : "+v"(rxo), "+v"(ppo));
      f32x2 wof[4][4];
#pragma unroll
      for (int r = 0; r < 4; ++r)
#pragma unroll
        for (int i = 0; i < 4; ++i) { if (i == 0) asm volatile("" : "+v"(wot[r])); wof[r][i] = f32x2{bflo(wot[r][i]), bfhi(wot[r][i])}; asm volatile("" : "+v"(wof[r][i])); }
      SCHED();
_Pragma("unroll 1")
      for (int spin = 0; spin < SPIN_CAP; ++spin) {
        u32 mk[8], mxw = umax(mlv.x, mlv.y);
#pragma unroll
        for (int k = 0; k < 8; ++k) { mk[k] = max4(pa[k]); mxw = umax(mxw, mk[k]); }
        if (__all(mxw != SENT)) break;
        u32 need = __any(umax(mlv.x, mlv.y) == SENT) ? 0x100u : 0u;
#pragma unroll
        for (int k = 0; k < 8; ++k) need |= __any(mk[k] == SENT) ? (1u << k) : 0u;
        CLOBBER();
        if (need & 0x100) mlv = __builtin_amdgcn_raw_buffer_load_b64(rsW, 1024 + (ln >> 1) * PA_REC + (ln & 1) * 8, base, AUX_SC1);
#pragma unroll
        for (int k = 0; k < 8; ++k)
          if (need & (1 << k)) pa[k] = ld128<AUX_SC1>(rsW, ln * 16, base + (8 * w + k) * PA_REC);
        SCHED();
      }
      __builtin_amdgcn_s_waitcnt(0x0F70);
      if (sp == 0 && w == 0 && ln == 0) __builtin_amdgcn_raw_buffer_store_b32(1u, rsW, 0, OFF_FL + (layer * 8 + kvh) * 128, AUX_SC1);
      STAMP(6 + 12 * layer);
      const float mm = u2f(mlv.x);
      float M = fmaxf(mm, dppf<XOR2>(mm));
      M = fmaxf(M, dppf<0x124>(M));
      M = fmaxf(M, dppf<ROR8>(M));
      M = xrow_max(M);
      const float wgt = __builtin_amdgcn_exp2f(mm - M);
      float L = wgt * u2f(mlv.y);
      L += dppf<XOR2>(L);
      L += dppf<0x124>(L);
      L += dppf<ROR8>(L);
      L = xrow_sum(L);
      const float fac = wgt * __builtin_amdgcn_rcpf(L);
      f32x4 o = {0.f, 0.f, 0.f, 0.f};
      const int src = (16 * w + ((ln >> 2) & 1)) * 4;
      float fk[8];
#pragma unroll
      for (int k = 0; k < 8; ++k) fk[k] = u2f((u32)__builtin_amdgcn_ds_bpermute(src + 8 * k, (int)f2u(fac)));
      SCHED();
#pragma unroll
      for (int k = 0; k < 8; ++k) o += fk[k] * __builtin_bit_cast(f32x4, pa[k]);
      f32x4* op = (f32x4*)(smem + L_MRG);
      op[w * 128 + ln] = o;
      STAMP(27 + layer);
      BAR();
      STAMP(29 + layer);
      const f32x4* opp = (const f32x4*)(smem + L_MRG + ppo);
      o = (opp[0] + opp[128]) + (opp[256] + opp[384]);
      float a[8];
#pragma unroll
      for (int i = 0; i < 4; ++i) {
        const f32x2 t = (o.x * wof[0][i] + o.y * wof[1][i]) + (o.z * wof[2][i] + o.w * wof[3][i]);
        a[2 * i] = t.x; a[2 * i + 1] = t.y;
      }
#pragma unroll
      for (int i = 0; i < 4; ++i) a[i] = swap32_add(a[i], a[i + 4]);
#pragma unroll
      for (int i = 0; i < 2; ++i) {
        a[i] = swap16_add(a[i], a[i + 2]);
        a[i] += dppf<ROR8>(a[i]);
        a[i] += dppf<0x124>(a[i]);
      }
      float* const rx = (float*)(smem + L_RX);
      if ((ln & 12) == 0) {
        float* const rp = (float*)(smem + L_RX + rxo);
        rp[0] = a[0];
        rp[4] = a[1];
      }
      BAR();
      if (w == 0 && ln < 32)
        __builtin_amdgcn_raw_buffer_store_b32(canon(f2u(sum4(*(const f32x4*)(rx + ln * 4)))), rsW, ln * 4, OFF_XB + (layer * 16 + rg) * 2048 + cs * 128, AUX_SC1);
    }
    STAMP(7 + 12 * layer);

    {
      v4u wt[4];
#pragma unroll
      for (int i = 0; i < 4; ++i) wt[i] = ldsW13[i * 256 + w * 64 + ln];
      f32x2 wf[2][8];
      unpack_w(wt, wf);
      float cs13 = sum4(*(const f32x4*)(red + 5 * RED_STRIDE + (ln & 15) * 4));
      f32x2 xi = ((const f32x2*)(smem + L_XI))[w * 64 + ln];
      const int ro = red_slot(w, ln);
      int own = w * 128 + 4 * (ln & 31) + 2 * (ln >> 5) - dcol;
      asm volatile("" : "+v"(cs13), "+v"(xi), "+v"(own));
_Pragma("unroll 1")
      for (int spin = 0; spin < SPIN_CAP; ++spin) {
        CLOBBER();
        const u32 fl = __builtin_amdgcn_raw_buffer_load_b32(rsW, (ln & 7) * 128, OFF_FL + layer * 1024, AUX_SC1);
        if (__all(fl != SENT || (ln & 7) == kvh)) break;
      }
      v4u g[8];
_Pragma("unroll 1")
      for (int spin = 0; spin < SPIN_CAP; ++spin) {
        CLOBBER();
#pragma unroll
        for (int k = 0; k < 8; ++k) g[k] = ld128<AUX_SC1>(rsW, (ln & 31) * 16 + (ln >> 5) * 2048, OFF_XB + layer * 32768 + w * 512 + k * 4096);
        u32 mxw = 0;
#pragma unroll
        for (int k = 0; k < 8; ++k) mxw = umax(mxw, max4(g[k]));
        if (__all(mxw != SENT)) break;
        __builtin_amdgcn_s_sleep(DLY_RETRY);
      }
      STAMP(8 + 12 * layer);
      const f32x4 sm = ((__builtin_bit_cast(f32x4, g[0]) + __builtin_bit_cast(f32x4, g[1])) + (__builtin_bit_cast(f32x4, g[2]) + __builtin_bit_cast(f32x4, g[3]))) +
                       ((__builtin_bit_cast(f32x4, g[4]) + __builtin_bit_cast(f32x4, g[5])) + (__builtin_bit_cast(f32x4, g[6]) + __builtin_bit_cast(f32x4, g[7])));
      const float x1a = xi.x + swap32_add(sm.x, sm.z), x1b = xi.y + swap32_add(sm.y, sm.w);
      if (own == 0) *(f32x2*)(red + 3 * RED_STRIDE) = f32x2{x1a, x1b};
      ln_gemv_part(x1a - cmean, x1b - cmean, wf, red + RED_STRIDE, ro, ln);
      BAR();
      float mu;
      const float gt = ln_gemv_final(red + RED_STRIDE, cs13, ln, mu);
      const float up = dppf<ROR8>(gt);
      const float hval = gt * __builtin_amdgcn_rcpf(1.0f + __expf(-gt)) * up;
      if (doS3 && w == 0 && ln < 8) __builtin_amdgcn_raw_buffer_store_b32(canon(f2u(hval)), rsW, ln * 4, OFF_H + layer * 8192 + s3 * 32, AUX_SC1);
      cmean = u2f((u32)__builtin_amdgcn_readfirstlane((int)f2u(cmean + mu)));
    }
    STAMP(9 + 12 * layer);

    {
      f32x2 w2f[7];
#pragma unroll
      for (int k = 0; k < 7; ++k) {
        u32 t;
        if (k < 4) t = ((const u32*)(smem + L_W2))[k * 256 + w * 64 + ln];
        else { asm volatile("" : "+v"(w2r[k])); t = w2r[k]; }
        w2f[k] = f32x2{bflo(t), bfhi(t)};
        asm volatile("" : "+v"(w2f[k]));
      }
      if constexpr (layer == 0) { SCHED(); static_for<0, NPRE / 2>(issue2); SCHED(); }
      __builtin_amdgcn_s_sleep(DLY_C1);
      u32 hv[7];
_Pragma("unroll 1")
      for (int spin = 0; spin < SPIN_CAP; ++spin) {
        CLOBBER();
        u32 mxw = 0;
#pragma unroll
        for (int k = 0; k < 7; ++k) hv[k] = __builtin_amdgcn_raw_buffer_load_b32(rsW, ln * 4, OFF_H + layer * 8192 + w * 256 + k * 1024, AUX_SC1);
#pragma unroll
        for (int k = 0; k < 7; ++k) mxw = umax(mxw, hv[k]);
        if (__all(mxw != SENT)) break;
        __builtin_amdgcn_s_sleep(DLY_RETRY);
      }
      STAMP(10 + 12 * layer);
      f32x2 a = u2f(hv[0]) * w2f[0];
#pragma unroll
      for (int k = 1; k < 7; ++k) a += u2f(hv[k]) * w2f[k];
      float r = swap32_add(a.x, a.y);
      r = swap16_add(r, r);
      r += dppf<XOR1>(r); r += dppf<XOR2>(r); r += dppf<HMIR>(r); r += dppf<MIR>(r);
      float* const rd = red + 2 * RED_STRIDE;
      if ((ln & 31) == 0) rd[(ln >> 5) * 4 + w] = r;
      BAR();
      if (w == 0 && ln == 0) {
        const f32x2 x1 = *(const f32x2*)(red + 3 * RED_STRIDE);
        const float y0 = x1.x + sum4(*(const f32x4*)rd), y1 = x1.y + sum4(*(const f32x4*)(rd + 4));
        if (layer == 0) __builtin_amdgcn_raw_buffer_store_b64(v2u{canon(f2u(y0)), canon(f2u(y1))}, rsW, 0, OFF_X2 + dcol * 4, AUX_SC1);
        else *(u32*)(p.out + dcol) = pack_bf16(y0, y1);
      }
    }
    STAMP(11 + 12 * layer);
    if constexpr (layer == 0) {

    {
      v4u wt[4];
#pragma unroll
      for (int i = 0; i < 4; ++i) wt[i] = ldsWq[i * 256 + w * 64 + ln];
      f32x2 wf[2][8];
      unpack_w(wt, wf);
      float csq = sum4(*(const f32x4*)(red + 4 * RED_STRIDE + (ln & 15) * 4));
      asm volatile("" : "+v"(csq));
      const int ro = red_slot(w, ln);
      SCHED(); static_for<NPRE / 2, NPRE>(issue2); SCHED();
      __builtin_amdgcn_s_sleep(DLY_C2);
      v2u g;
_Pragma("unroll 1")
      for (int spin = 0; spin < SPIN_CAP; ++spin) {
        CLOBBER();
        g = __builtin_amdgcn_raw_buffer_load_b64(rsW, (ln & 31) * 16 + (ln >> 5) * 8, OFF_X2 + w * 512, AUX_SC1);
        if (__all(umax(g.x, g.y) != SENT)) break;
        __builtin_amdgcn_s_sleep(DLY_RETRY);
      }
      STAMP(12);
      const float xia = u2f(g.x), xib = u2f(g.y);
      ((v2u*)(smem + L_XI))[w * 64 + ln] = g;
      ln_gemv_part(xia - cmean, xib - cmean, wf, red, ro, ln);
      BAR();
      float mu;
      const float qv = ln_gemv_final(red, csq, ln, mu);
      const u32 pk = canon(pack_bf16(qv, dppf<XOR1>(qv)));
      if (w == 0 && ln < 16 && (ln & 1) == 0) __builtin_amdgcn_raw_buffer_store_b32(pk, rsW, ln * 2, OFF_Q1 + qcol * 8, AUX_SC1);
      cmean = u2f((u32)__builtin_amdgcn_readfirstlane((int)f2u(cmean + mu)));
      STAMP(13);
    }
    int qo = voffK;
    asm volatile("" : "+v"(qo));
    qo = qoff(qo);
    __builtin_amdgcn_s_sleep(DLY_D);
_Pragma("unroll 1")
    for (int spin = 0; spin < SPIN_CAP; ++spin) {
      CLOBBER();
      qf = ld128<AUX_SC1>(rsW, qo, OFF_Q1 + kvh * 4096);
      if (__all(max4(qf) != SENT)) break;
    }
    }
  };
  layer_body(IC<0>{});
  layer_body(IC<1>{});
  asm volatile(".Ldk_code_end:");
}

extern "C" void kernel_launch(void* const* d_in, const int* in_sizes, int n_in, void* d_out, int out_size,
                              void* d_ws, size_t ws_size, hipStream_t stream) {
  Params p;
  p.x = (const u16*)d_in[0];
  p.K = (const u16*)d_in[1];
  p.V = (const u16*)d_in[2];
  p.wq = (const u16*)d_in[3];
  p.wo = (const u16*)d_in[4];
  p.w1 = (const u16*)d_in[6];
  p.w3 = (const u16*)d_in[7];
  p.w2 = (const u16*)d_in[8];
  p.out = (u16*)d_out;
  p.ws = (unsigned char*)d_ws;
  decode_kernel<<<256, 256, 0, stream>>>(p);
}
```

```cpp
#include <hip/hip_runtime.h>
#include <stdint.h>

typedef unsigned u32;
typedef unsigned long long u64;
typedef unsigned short u16;
typedef u32 v4u __attribute__((ext_vector_type(4)));
typedef u32 v2u __attribute__((ext_vector_type(2)));
typedef float f32x4 __attribute__((ext_vector_type(4)));
typedef float f32x2 __attribute__((ext_vector_type(2)));
typedef __bf16 bf16x2 __attribute__((ext_vector_type(2)));
typedef __amdgpu_buffer_rsrc_t rsrc_t;

constexpr int AUX_NT = 2;
constexpr int AUX_SC1 = 16;
constexpr u32 SENT = 0xFFFFFFFFu;
constexpr int SPIN_CAP = 200000;

constexpr int OFF_EPOCH = 0;
constexpr int OFF_READY = 4096;
constexpr int OFF_Q0 = 8192;
constexpr int OFF_HO = OFF_Q0 + 32768;
constexpr int PA_REC = 1152;
constexpr int OFF_PA = OFF_HO;
constexpr int OFF_XB = OFF_PA + 2 * 16 * 32 * PA_REC;
constexpr int OFF_H = OFF_XB + 2 * 16 * 2048;
constexpr int OFF_X2 = OFF_H + 2 * 8192;
constexpr int OFF_FL = OFF_X2 + 2048;
constexpr int OFF_Q1 = OFF_FL + 2048;
constexpr int HO_USED = OFF_Q1 + 32768 - OFF_HO;
constexpr int HO_SLICE = 5120;
static_assert(HO_USED <= 256 * HO_SLICE, "reset slices must cover the hand-off region");
constexpr int WS_BYTES = OFF_HO + 256 * HO_SLICE;

constexpr int RED_STRIDE = 80;
constexpr int L_RED = 0;
constexpr int L_ML = L_RED + 6 * RED_STRIDE * 4;
constexpr int L_MRG = L_ML + 128;
constexpr int L_RX = L_MRG + 1024;
constexpr int L_XI = L_MRG + 8192;
constexpr int L_W2 = L_XI + 2048;
constexpr int L_WQ = L_W2 + 4096;
constexpr int L_W13 = L_WQ + 16384;
constexpr int L_V = L_W13 + 16384;
constexpr int L_BYTES = L_V + 114688;
static_assert(L_BYTES <= 163840 && L_V % 16 == 0, "LDS");

constexpr int DLY_A = 16;
constexpr int DLY_C1 = 12;
constexpr int DLY_C2 = 12;
constexpr int DLY_D = 5;
constexpr int DLY_RETRY = 3;

struct Params {
  const u16* x; const u16* K; const u16* V; const u16* wq; const u16* wo;
  const u16* w1; const u16* w3; const u16* w2; u16* out; unsigned char* ws;
};

__device__ __forceinline__ float u2f(u32 x) { return __builtin_bit_cast(float, x); }
__device__ __forceinline__ u32 f2u(float x) { return __builtin_bit_cast(u32, x); }
__device__ __forceinline__ float bflo(u32 x) { return u2f(x << 16); }
__device__ __forceinline__ float bfhi(u32 x) { return u2f(x & 0xffff0000u); }
__device__ __forceinline__ u32 pack_bf16(float lo, float hi) {
  bf16x2 r = {(__bf16)lo, (__bf16)hi};
  return __builtin_bit_cast(u32, r);
}
__device__ __forceinline__ u32 canon(u32 x) { return x < SENT - 1u ? x : SENT - 1u; }
template <int CTRL> __device__ __forceinline__ float dppf(float x) {
  return u2f((u32)__builtin_amdgcn_mov_dpp((int)f2u(x), CTRL, 0xf, 0xf, true));
}
template <int CTRL> __device__ __forceinline__ u32 dppu(u32 x) {
  return (u32)__builtin_amdgcn_mov_dpp((int)x, CTRL, 0xf, 0xf, true);
}
constexpr int XOR1 = 0xB1, XOR2 = 0x4E, XOR3 = 0x1B, HMIR = 0x141, MIR = 0x140, ROR8 = 0x128;
__device__ __forceinline__ float swap32_add(float a, float b) {
  auto r = __builtin_amdgcn_permlane32_swap(f2u(a), f2u(b), false, false);
  return u2f(r[0]) + u2f(r[1]);
}
__device__ __forceinline__ float swap16_add(float a, float b) {
  auto r = __builtin_amdgcn_permlane16_swap(f2u(a), f2u(b), false, false);
  return u2f(r[0]) + u2f(r[1]);
}
__device__ __forceinline__ float xrow_max(float x) {
  auto s = __builtin_amdgcn_permlane16_swap(f2u(x), f2u(x), false, false);
  x = fmaxf(u2f(s[0]), u2f(s[1]));
  auto t = __builtin_amdgcn_permlane32_swap(f2u(x), f2u(x), false, false);
  return fmaxf(u2f(t[0]), u2f(t[1]));
}
__device__ __forceinline__ float xrow_sum(float x) {
  x = swap16_add(x, x);
  return swap32_add(x, x);
}
__device__ __forceinline__ float treduce16(float (&v)[16], int lane) {
#pragma unroll
  for (int i = 0; i < 8; ++i) v[i] = swap32_add(v[i], v[i + 8]);
#pragma unroll
  for (int i = 0; i < 4; ++i) v[i] = swap16_add(v[i], v[i + 4]);
  const bool b3 = lane & 8, b2 = lane & 4;
#pragma unroll
  for (int i = 0; i < 2; ++i) {
    const float keep = b3 ? v[i + 2] : v[i], send = b3 ? v[i] : v[i + 2];
    v[i] = keep + dppf<ROR8>(send);
  }
  const float keep = b2 ? v[1] : v[0], send = b2 ? v[0] : v[1];
  float r = keep + dppf<HMIR>(send);
  r += dppf<XOR2>(r);
  r += dppf<XOR1>(r);
  return r;
}
__device__ __forceinline__ f32x4 qk1(v4u k, v4u q) {
  f32x4 d;
  asm volatile("v_mfma_f32_16x16x32_bf16 %0, %1, %2, 0" : "=&v"(d) : "v"(k), "v"(q));
  return d;
}
__device__ __forceinline__ void qk_fence(f32x4 (&D)[4]) {
  asm volatile("s_nop 7" : "+v"(D[0]), "+v"(D[1]), "+v"(D[2]), "+v"(D[3]));
}
__device__ __forceinline__ void pv2a(f32x4& a0, f32x4& a1, v4u va, v4u vb, v4u pv) {
  asm volatile("s_nop 1\n\t"
      "v_mfma_f32_16x16x32_bf16 %0, %2, %4, %0\n\t"
      "v_mfma_f32_16x16x32_bf16 %1, %3, %4, %1"
      : "+v"(a0), "+v"(a1) : "a"(va), "a"(vb), "v"(pv));
}
__device__ __forceinline__ void pv2v(f32x4& a0, f32x4& a1, v4u va, v4u vb, v4u pv) {
  asm volatile("s_nop 1\n\t"
      "v_mfma_f32_16x16x32_bf16 %0, %2, %4, %0\n\t"
      "v_mfma_f32_16x16x32_bf16 %1, %3, %4, %1"
      : "+v"(a0), "+v"(a1) : "v"(va), "v"(vb), "v"(pv));
}
__device__ __forceinline__ v4u ldw(const void* base, u32 off) {
  return *(const v4u*)((const unsigned char*)base + off);
}
constexpr int home(int T, int es) { return T >= 12 ? 2 : T == 11 ? (es < 2 ? 1 : 2) : (T % 3 == 2 ? 1 : 0); }
constexpr int hidx(int T, int es) { return T >= 12 ? T - 12 : T == 11 ? (es < 2 ? 3 : 4) : (T % 3 == 2 ? T / 3 : T - T / 3); }
constexpr int lslot(int T, int e) { return T == 11 ? 24 + (e >> 2) * 2 + (e & 3) : (T / 3) * 8 + e; }
template <int AUX> __device__ __forceinline__ v4u ld128(rsrc_t rs, int voff, int soff) {
  return __builtin_amdgcn_raw_buffer_load_b128(rs, voff, soff, AUX);
}
__device__ __forceinline__ int chunk(int j, int rot4k) { return (j * 4096 + rot4k) & 0x7FFFF; }
constexpr int RD = 3;
constexpr int MALLM = 5;
constexpr int MALLN = 2;
constexpr int NPRE3 = 4;
constexpr int NPRE = 8 + NPRE3;
constexpr int order2(int st) { return st == 0 ? 12 : st < 7 ? ((st & 1) ? st / 2 : 12 + st / 2) : st == 14 ? 11 : st == 15 ? 10 : st - 4; }
constexpr int blk2(int i) {
  const int u = i < 8 ? i : i < NPRE ? i + 8 : i < 16 + NPRE3 ? i - NPRE3 : i;
  return 8 * order2(u >> 3) + (u & 7);
}
template <bool FIRST> __device__ __forceinline__ v4u ldk(rsrc_t rs, int voff, int j, int rot4k) {
  if (FIRST && (j >= 96 || j % MALLM < MALLN)) return ld128<0>(rs, voff, chunk(j, rot4k));
  return ld128<AUX_NT>(rs, voff, chunk(j, rot4k));
}
constexpr int W1 = 18;
constexpr int win2(int i) { return i < 8 ? 10 + NPRE3 : i < 24 ? 18 + NPRE3 : i < 48 ? 20 : 12; }
constexpr int cmin(int a, int b) { return a < b ? a : b; }
constexpr int cmax(int a, int b) { return a > b ? a : b; }
constexpr int hi_start(int st);
constexpr int hi_after(int i) { return i < 0 ? NPRE : cmax((i & 7) ? hi_after(i - 1) : hi_start(i >> 3), cmin(128, i + 1 + win2(i))); }
constexpr int hi_start(int st) { return cmax(hi_after(8 * st - 1), cmin(128, 8 * st + win2(8 * st))); }
__device__ __forceinline__ int qoff(int voffK) { return ((voffK >> 5) & 31) * 128 + (voffK & 31); }
__device__ __forceinline__ u32 umax(u32 a, u32 b) { return a > b ? a : b; }
__device__ __forceinline__ u32 max4(v4u v) { return umax(umax(v.x, v.y), umax(v.z, v.w)); }
#define BAR() asm volatile("s_waitcnt lgkmcnt(0)\n\ts_barrier" ::: "memory")
#define CLOBBER() asm volatile("" ::: "memory")
#define SCHED() __builtin_amdgcn_sched_barrier(0)
__device__ __forceinline__ void unpack_w(const v4u (&wt)[4], f32x2 (&wf)[2][8]) {
#pragma unroll
  for (int r = 0; r < 2; ++r)
#pragma unroll
    for (int i = 0; i < 4; ++i) {
      wf[r][i] = f32x2{bflo(wt[r][i]), bfhi(wt[r][i])};
      wf[r][4 + i] = f32x2{bflo(wt[2 + r][i]), bfhi(wt[2 + r][i])};
    }
#pragma unroll
  for (int r = 0; r < 2; ++r)
#pragma unroll
    for (int i = 0; i < 8; ++i) asm volatile("" : "+v"(wf[r][i]));
}
__device__ __forceinline__ int red_slot(int w, int lane) {
  int o = (lane & 3) == 0 ? (lane + w) * 4 : (lane & 31) == 1 ? ((16 + (lane >> 5)) * 4 + w) * 4 : -1;
  asm volatile("" : "+v"(o));
  return o;
}
__device__ __forceinline__ void ln_gemv_part(float da, float db, const f32x2 (&wf)[2][8], float* red, int ro, int lane) {
  float a[16];
#pragma unroll
  for (int i = 0; i < 8; ++i) {
    const f32x2 t = da * wf[0][i] + db * wf[1][i];
    a[2 * i] = t.x; a[2 * i + 1] = t.y;
  }
  float st = swap32_add(da + db, fmaf(da, da, db * db));
  st = swap16_add(st, st);
  st += dppf<XOR1>(st); st += dppf<XOR2>(st); st += dppf<HMIR>(st); st += dppf<MIR>(st);
  const float r = treduce16(a, lane);
  float v = (lane & 1) ? st : r;
  asm volatile("" : "+v"(v));
  if (ro >= 0) *(float*)((unsigned char*)red + ro) = v;
}
__device__ __forceinline__ float sum4(f32x4 v) { return (v.x + v.y) + (v.z + v.w); }
__device__ __forceinline__ float ln_gemv_final(const float* red, float cs, int lane, float& mu) {
  const f32x4 g = *(const f32x4*)(red + (lane & 15) * 4), s1 = *(const f32x4*)(red + 64), s2 = *(const f32x4*)(red + 68);
  mu = sum4(s1) * (1.0f / 512.0f);
  const float var = fmaf(-mu, mu, sum4(s2) * (1.0f / 512.0f));
  return fmaf(-mu, cs, sum4(g)) * __builtin_amdgcn_rsqf(fmaxf(var, 0.f) + 1e-5f);
}

template <int I> struct IC { static constexpr int value = I; };
template <int I, int N, class F> __device__ __forceinline__ void static_for(F&& f) {
  if constexpr (I < N) { f(IC<I>{}); static_for<I + 1, N>(f); }
}

__global__ void __launch_bounds__(256, 1) decode_kernel(Params p) {
  __shared__ __attribute__((aligned(16))) unsigned char smem[L_BYTES];
  const int tid = threadIdx.x, lane = tid & 63, w = __builtin_amdgcn_readfirstlane(tid >> 6);
  const int b = blockIdx.x, kvh = b & 7, sp = b >> 3;
  const rsrc_t rsW = __builtin_amdgcn_make_buffer_rsrc((void*)p.ws, 0, WS_BYTES, 0x00020000);
  const size_t kvoff = ((size_t)kvh * 65536 + (size_t)sp * 2048) * 128;
  const rsrc_t rsK = __builtin_amdgcn_make_buffer_rsrc((void*)(p.K + kvoff), 0, 2048 * 256, 0x00020000);
  const rsrc_t rsV = __builtin_amdgcn_make_buffer_rsrc((void*)(p.V + kvoff), 0, 2048 * 256, 0x00020000);

  float* const red = (float*)(smem + L_RED);
  u64 code0;
  u32 code_len;
  asm volatile("s_getpc_b64 %0\n.Ldk_code_begin:\n\ts_mov_b32 %1, .Ldk_code_end-.Ldk_code_begin" : "=s"(code0), "=s"(code_len));
  const rsrc_t rsC = __builtin_amdgcn_make_buffer_rsrc((void*)code0, 0, code_len, 0x00020000);

  const int rot4k = ((sp * 5 + kvh * 16) & 127) * 4096;
  int rotK1 = rot4k, rotK2 = rot4k;
  asm volatile("" : "+s"(rotK1));
  asm volatile("" : "+s"(rotK2));
  const int rgw = 2 * kvh + (sp & 1);
  const int voffV = lane * 16 + w * 1024;
  const int voffK = ((lane >> 2) & 3) * 256 + ((lane >> 4) * 4 + (lane & 3)) * 16 + w * 1024;
  v4u vresA[8][8], vresV[5][8];
  v4u raw[RD][8], rawL[2][8];
  v4u k1[128], k2[128];
#pragma unroll
  for (int T = 0; T < RD; ++T)
#pragma unroll
    for (int t = 0; t < 8; ++t) raw[T][t] = ld128<AUX_NT>(rsV, voffV, chunk(8 * T + t, rot4k));
  SCHED();
  typedef const __attribute__((address_space(4))) u32* cptr;
  u32 warm[12];
  {
    warm[0] = *(cptr)((const unsigned char*)p.wq + 0); warm[1] = *(cptr)((const unsigned char*)p.wq + (2 << 20) + 64); warm[2] = *(cptr)((const unsigned char*)p.wq + (4 << 20) - 64);
    warm[3] = *(cptr)((const unsigned char*)p.w1 + 0); warm[4] = *(cptr)((const unsigned char*)p.w1 + 512 * 3584 - 64);
    warm[5] = *(cptr)((const unsigned char*)p.w3 + 0); warm[6] = *(cptr)((const unsigned char*)p.w3 + 512 * 3584 - 64);
    warm[7] = *(cptr)((const unsigned char*)p.w2 + 0); warm[8] = *(cptr)((const unsigned char*)p.w2 + 1792 * 1024 - 64);
    warm[9] = *(cptr)((const unsigned char*)p.wo + rgw * 262144);
    warm[10] = *(cptr)(p.K + kvoff);
    warm[11] = *(cptr)p.x;
    SCHED();
  }
  u32 touch[5];
  touch[0] = *(const u32*)((const unsigned char*)p.wq + (u32)((16 * sp + 4 * w + (lane >> 4)) * 8192 + kvh * 1024 + (lane & 15) * 64));
  touch[3] = __builtin_amdgcn_raw_buffer_load_b32(rsC, tid * 64, sp * 16384, 0);
  if (w == 0) {
    const v4u s4 = {SENT, SENT, SENT, SENT};
#pragma unroll
    for (int i = 0; i < 5; ++i) __builtin_amdgcn_raw_buffer_store_b128(s4, rsW, OFF_HO + lane * 16 + i * 1024, b * HO_SLICE, AUX_SC1);
  }
  const v2u ep = __builtin_amdgcn_raw_buffer_load_b64(rsW, OFF_EPOCH, 0, AUX_SC1);
  const int row0 = w * 128 + 4 * (lane & 31) + 2 * (lane >> 5);
  const int qcol = kvh * 512 + sp * 16;
  const int rg = 2 * kvh + (sp & 1), cs = sp >> 1;
  const bool doS3 = sp < 28;
  const int s3 = kvh * 28 + (doS3 ? sp : 0);
  const int dcol = kvh * 64 + sp * 2;
  u32 x01 = 0, x00 = 0;
  SCHED();

  float cmean = 0.f;
  u32 tag_lo = 0, tag_hi = 0;
  u32 w2r[7];
  v4u qf = {0u, 0u, 0u, 0u}, r0 = qf, r1 = qf;
  v4u wtmp[8], wot[4];
  v4u* const ldsV = (v4u*)(smem + L_V) + w * (28 * 64) + lane;
  v4u* const ldsWq = (v4u*)(smem + L_WQ);
  v4u* const ldsW13 = (v4u*)(smem + L_W13);
  auto ingest = [&](auto TC, v4u (&rw)[8], auto&& between) __attribute__((always_inline)) {
    constexpr int T = decltype(TC)::value;
    v4u f[8];
#pragma unroll
    for (int e = 0; e < 8; ++e)
#pragma unroll
      for (int j = 0; j < 4; ++j)
        f[e][j] = __builtin_amdgcn_perm(rw[j + 4][e >> 1], rw[j][e >> 1], (e & 1) ? 0x07060302u : 0x05040100u);
#pragma unroll
    for (int e = 0; e < 8; ++e)
      if (home(T, e & 3) == 2) asm volatile("" : "+v"(f[e]));
    SCHED();
    between();
#pragma unroll
    for (int e = 0; e < 8; ++e) {
      if (home(T, e & 3) == 1) ldsV[lslot(T, e) * 64] = f[e];
      else if (home(T, e & 3) == 2) vresV[hidx(T, e & 3)][e] = f[e];
      else { asm volatile("" : "+a"(f[e])); vresA[hidx(T, e & 3)][e] = f[e]; }
    }
    SCHED();
  };
  auto issue1 = [&](auto UC) __attribute__((always_inline)) {
    constexpr int u = decltype(UC)::value;
    if constexpr (u < 112) k1[u] = ldk<true>(rsK, voffK, u, rotK1);
    else if constexpr (u < 120) rawL[0][u - 112] = ld128<AUX_NT>(rsV, voffV, chunk(u, rot4k));
    else if constexpr (u < 128) k1[u - 8] = ldk<true>(rsK, voffK, u - 8, rotK1);
    else if constexpr (u < 136) rawL[1][u - 128] = ld128<AUX_NT>(rsV, voffV, chunk(u - 8, rot4k));
    else if constexpr (u < 144) k1[u - 16] = ldk<true>(rsK, voffK, u - 16, rotK1);
  };
  static_for<0, 14>([&](auto TC) __attribute__((always_inline)) {
    constexpr int T = decltype(TC)::value;
    ingest(TC, raw[T % RD], [&]() __attribute__((always_inline)) {
    __builtin_amdgcn_s_barrier();
    SCHED();
    if constexpr (T + RD < 14) {
#pragma unroll
      for (int t = 0; t < 8; ++t) raw[T % RD][t] = ld128<AUX_NT>(rsV, voffV, chunk(8 * (T + RD) + t, rot4k));
    } else {
      static_for<(T - 14 + RD) * W1 / RD, (T - 13 + RD) * W1 / RD>(issue1);
    }
    if constexpr (T == 0) {
      asm volatile("" ::"s"(warm[0]), "s"(warm[1]), "s"(warm[2]), "s"(warm[3]), "s"(warm[4]), "s"(warm[5]), "s"(warm[6]), "s"(warm[7]), "s"(warm[8]), "s"(warm[9]), "s"(warm[10]), "s"(warm[11]));
      const int l7 = lane < 56 ? lane : 55;
      touch[1] = *(const u32*)((const unsigned char*)(w < 2 ? p.w1 : p.w3) + (u32)((16 * sp + 8 * (w & 1) + l7 / 7) * 3584 + kvh * 448 + (l7 % 7) * 64));
    }
    if constexpr (T == 1) {
      x01 = *(const u32*)(p.x + row0);
      x00 = *(const u32*)p.x;
      const int l2 = lane < 28 ? lane : 27;
      touch[2] = *(const u32*)((const unsigned char*)p.w2 + (u32)((56 * sp + 14 * w + (l2 >> 1)) * 1024 + kvh * 128 + (l2 & 1) * 64));
    }
    if constexpr (T == 1 || T == 2) {
#pragma unroll
      for (int i = 2 * (T - 1); i < 2 * (T - 1) + 2; ++i) wtmp[i] = ldw(p.wq, (w * 128 + 32 * i + (lane >> 1)) * 8192 + qcol * 2 + (lane & 1) * 16);
    }
    if constexpr (T >= 3 && T < 7) wtmp[T + 1] = ldw(T < 5 ? p.w1 : p.w3, (row0 + ((T + 1) & 1)) * 3584 + s3 * 16);
    if constexpr (T >= 5 && T < 12) w2r[T - 5] = *(const u32*)((const unsigned char*)p.w2 + (u32)((tid + 256 * (T - 5)) * 1024 + dcol * 2));
    if constexpr (T >= 7 && T < 11) {
      const int pp = 16 * w + (lane >> 2);
      const int orow = rg * 256 + ((pp >> 2) & 1) * 128 + ((pp >> 3) & 3) * 32 + (pp >> 5) * 4 + (pp & 3);
      wot[T - 7] = ldw(p.wo, (orow + 8 * (T - 7)) * 1024 + cs * 64 + (lane & 3) * 16);
    }
    if constexpr (T == 9) touch[4] = *(const u32*)(p.out + 2 * lane);
    if constexpr (T >= 8 && T < 12) ((u32*)(smem + L_W2))[(T - 8) * 256 + tid] = w2r[T - 8];
    if constexpr (T == 5) asm volatile("" ::"v"(touch[0]), "v"(touch[1]), "v"(touch[2]), "v"(touch[3]));
    if constexpr (T == 13) asm volatile("" ::"v"(touch[4]));
    });
    if constexpr (T == 5) {
#pragma unroll
      for (int i = 0; i < 4; ++i) {
        const int rho = 32 * i + (lane >> 1);
        ldsWq[((lane & 1) * 2 + (rho & 1)) * 256 + w * 64 + (rho >> 2) + 32 * ((rho >> 1) & 1)] = wtmp[i];
      }
      v4u wt[4];
#pragma unroll
      for (int i = 0; i < 4; ++i) wt[i] = ldsWq[i * 256 + tid];
      f32x2 wf[2][8];
      unpack_w(wt, wf);
      const float xia = bflo(x01), xib = bfhi(x01);
      ((f32x2*)(smem + L_XI))[tid] = f32x2{xia, xib};
      cmean = bflo(x00);
      const int ro = red_slot(w, lane);
      ln_gemv_part(1.0f, 1.0f, wf, red + 4 * RED_STRIDE, ro, lane);
      ln_gemv_part(xia - cmean, xib - cmean, wf, red, ro, lane);
      BAR();
      const float csq = sum4(*(const f32x4*)(red + 4 * RED_STRIDE + (lane & 15) * 4));
      float mu;
      const float qv = ln_gemv_final(red, csq, lane, mu);
      cmean = u2f((u32)__builtin_amdgcn_readfirstlane((int)f2u(cmean + mu)));
      const u32 pk = canon(pack_bf16(qv, dppf<XOR1>(qv)));
      if (w == 0 && lane < 16 && (lane & 1) == 0) __builtin_amdgcn_raw_buffer_store_b32(pk, rsW, lane * 2, OFF_Q0 + qcol * 8, AUX_SC1);
      const u64 epoch = (((u64)ep.y << 32) | ep.x) + 0x9E3779B97F4A7C15ull;
      tag_lo = (u32)__builtin_amdgcn_readfirstlane((int)(u32)epoch);
      tag_hi = (u32)__builtin_amdgcn_readfirstlane((int)(u32)(epoch >> 32));
      SCHED();
    }
    if constexpr (T == 7) if (w == 0) {
      asm volatile("s_waitcnt vmcnt(16)" ::: "memory");
      if (lane == 0) __builtin_amdgcn_raw_buffer_store_b64(v2u{tag_lo, tag_hi}, rsW, 0, OFF_READY + b * 8, AUX_SC1);
      SCHED();
    }
    if constexpr (T == 9) {
      const v4u wt[4] = {wtmp[4], wtmp[5], wtmp[6], wtmp[7]};
#pragma unroll
      for (int i = 0; i < 4; ++i) ldsW13[i * 256 + tid] = wt[i];
      f32x2 wf[2][8];
      unpack_w(wt, wf);
      ln_gemv_part(1.0f, 1.0f, wf, red + 5 * RED_STRIDE, red_slot(w, lane), lane);
      SCHED();
    }
    if constexpr (T == 11) {
      r0 = ld128<AUX_SC1>(rsW, OFF_READY + lane * 16, 0);
      r1 = ld128<AUX_SC1>(rsW, OFF_READY + 1024 + lane * 16, 0);
      SCHED();
    }
    if constexpr (T == 13) {
      auto not_ready = [&](v4u a, v4u c) {
        const u32 bad = ((a.x ^ tag_lo) | (a.y ^ tag_hi) | (a.z ^ tag_lo) | (a.w ^ tag_hi)) |
                        ((c.x ^ tag_lo) | (c.y ^ tag_hi) | (c.z ^ tag_lo) | (c.w ^ tag_hi));
        return __any(bad != 0);
      };
      if (__builtin_expect(not_ready(r0, r1), 0)) {
_Pragma("unroll 1")
        for (int spin = 0; spin < SPIN_CAP; ++spin) {
          __builtin_amdgcn_s_sleep(8);
          CLOBBER();
          const v4u a = ld128<AUX_SC1>(rsW, OFF_READY + lane * 16, 0), c = ld128<AUX_SC1>(rsW, OFF_READY + 1024 + lane * 16, 0);
          if (!not_ready(a, c)) break;
        }
      }
      CLOBBER();
      qf = ld128<AUX_SC1>(rsW, qoff(voffK), OFF_Q0 + kvh * 4096);
      if (b == 0 && tid == 0) __builtin_amdgcn_raw_buffer_store_b64(v2u{tag_lo, tag_hi}, rsW, OFF_EPOCH, 0, AUX_SC1);
      SCHED();
    }
  });

  const int c4 = lane & 3;
  const bool cb0 = lane & 1, cb1 = lane & 2;
  constexpr float C2 = 0.08838834764831845f * 1.4426950408889634f;

  auto issue2 = [&](auto IC_) __attribute__((always_inline)) {
    constexpr int i = decltype(IC_)::value;
    k2[blk2(i)] = ldk<false>(rsK, voffK, blk2(i), rotK2);
  };
  auto layer_body = [&](auto LC) __attribute__((always_inline)) {
    constexpr int layer = decltype(LC)::value;
    float m = -INFINITY, mc = 0.f, l = 0.f;
    f32x4 acc[2] = {f32x4{0.f, 0.f, 0.f, 0.f}, f32x4{0.f, 0.f, 0.f, 0.f}};
    u32 junk = 0u;
    int ln = lane;
    asm volatile("s_nop 1" : "+v"(qf));
    static_for<0, 16>([&](auto SC) __attribute__((always_inline)) {
      constexpr int st = decltype(SC)::value;
      constexpr int T = layer ? order2(st) : st;
      if constexpr (layer == 1) { static_for<hi_after(8 * st - 1), hi_start(st)>(issue2); SCHED(); }
      if constexpr (layer == 0 && T >= 14)
        ingest(IC<T>{}, rawL[T - 14], [&]() __attribute__((always_inline)) { static_for<112 + 16 * (T - 14) + W1, 120 + 16 * (T - 14) + W1>(issue1); });
      if constexpr (layer == 0 && st == 13) { junk = __builtin_amdgcn_raw_buffer_load_b32(rsC, tid * 64, sp * 16384, AUX_SC1); SCHED(); }
      if constexpr (layer == 0 && st == 15) { asm volatile("" ::"v"(junk)); SCHED(); }
      float X[2];
      static_for<0, 2>([&](auto HC) __attribute__((always_inline)) {
        constexpr int hf = decltype(HC)::value;
        f32x4 D[4];
        static_for<0, 4>([&](auto QC) __attribute__((always_inline)) {
          constexpr int t = decltype(QC)::value, blk = 8 * T + 4 * hf + t, i = 8 * st + 4 * hf + t;
          D[t] = qk1(layer ? k2[blk] : k1[blk], qf);
          if constexpr (layer == 0) issue1(IC<(blk < 112 ? blk : blk < 120 ? blk + 8 : blk + 16) + W1>{});
          else static_for<((i & 7) ? hi_after(i - 1) : hi_start(st)), hi_after(i)>(issue2);
          SCHED();
        });
        qk_fence(D);
        SCHED();
        const float e0 = cb1 ? (cb0 ? D[3][3] : D[2][2]) : (cb0 ? D[1][1] : D[0][0]);
        const float e1 = cb1 ? (cb0 ? D[2][3] : D[3][2]) : (cb0 ? D[0][1] : D[1][0]);
        const float e2 = cb1 ? (cb0 ? D[1][3] : D[0][2]) : (cb0 ? D[3][1] : D[2][0]);
        const float e3 = cb1 ? (cb0 ? D[0][3] : D[1][2]) : (cb0 ? D[2][1] : D[3][0]);
        X[hf] = (e0 + dppf<XOR1>(e1)) + (dppf<XOR2>(e2) + dppf<XOR3>(e3));
      });
      float mx = fmaxf(X[0], X[1]);
      if (__any(mx > m)) {
        mx = fmaxf(mx, dppf<XOR1>(mx));
        mx = fmaxf(mx, dppf<XOR2>(mx));
        mx = xrow_max(mx);
        const float mn = fmaxf(m, mx);
        const float mcn = (mn == -INFINITY) ? 0.f : mn * C2;
        const float alpha = __builtin_amdgcn_exp2f(m * C2 - mcn);
        l *= alpha;
        acc[0] *= alpha; acc[1] *= alpha;
        m = mn; mc = mcn;
      }
      const float p0 = __builtin_amdgcn_exp2f(fmaf(X[0], C2, -mc)), p1 = __builtin_amdgcn_exp2f(fmaf(X[1], C2, -mc));
      l += p0 + p1;
      const u32 Y = pack_bf16(p0, p1);
      const v4u Yb = {dppu<0x00>(Y), dppu<0x55>(Y), dppu<0xAA>(Y), dppu<0xFF>(Y)};
#pragma unroll
      for (int es = 0; es < 4; ++es) {
        const bool on = c4 == es;
        const v4u pv = {on ? Yb.x : 0u, on ? Yb.y : 0u, on ? Yb.z : 0u, on ? Yb.w : 0u};
        if (home(T, es) == 0) pv2a(acc[0], acc[1], vresA[hidx(T, es)][es], vresA[hidx(T, es)][4 + es], pv);
        else if (home(T, es) == 2) pv2v(acc[0], acc[1], vresV[hidx(T, es)][es], vresV[hidx(T, es)][4 + es], pv);
        else pv2v(acc[0], acc[1], ldsV[lslot(T, es) * 64], ldsV[lslot(T, 4 + es) * 64], pv);
      }
      SCHED();
    });
    asm volatile("" : "+v"(ln));

    l += dppf<XOR1>(l); l += dppf<XOR2>(l); l = xrow_sum(l);
    asm volatile("s_nop 7" : "+v"(acc[0]), "+v"(acc[1]));
    {
      f32x4* mrg = (f32x4*)(smem + L_MRG);
      mrg[(w * 2 + 0) * 64 + ln] = acc[0];
      mrg[(w * 2 + 1) * 64 + ln] = acc[1];
      f32x2* ml = (f32x2*)(smem + L_ML);
      if (ln < 16 && (ln & 3) == 0) ml[(ln >> 2) * 4 + w] = f32x2{mc, l};
      BAR();
      if (w < 2) {
        const int h = (ln >> 2) & 3;
        const f32x4 qa = *(const f32x4*)(ml + h * 4), qb = *(const f32x4*)(ml + h * 4 + 2);
        const float M = fmaxf(fmaxf(qa.x, qa.z), fmaxf(qb.x, qb.z));
        const float f0 = __builtin_amdgcn_exp2f(qa.x - M), f1 = __builtin_amdgcn_exp2f(qa.z - M);
        const float f2 = __builtin_amdgcn_exp2f(qb.x - M), f3 = __builtin_amdgcn_exp2f(qb.z - M);
        const f32x4 o = (f0 * mrg[w * 64 + ln] + f1 * mrg[(2 + w) * 64 + ln]) + (f2 * mrg[(4 + w) * 64 + ln] + f3 * mrg[(6 + w) * 64 + ln]);
        const float L = fmaf(f0, qa.y, f1 * qa.w) + fmaf(f2, qb.y, f3 * qb.w);
        const int pos = (w * 32 + (ln >> 4) * 8 + (h & 1) * 4 + (ln & 3)) * 16 + (h >> 1) * (32 * PA_REC);
        const int rec = OFF_PA + ((layer * 16 + 2 * kvh) * 32 + sp) * PA_REC;
        const v4u ov = {canon(f2u(o.x)), canon(f2u(o.y)), canon(f2u(o.z)), canon(f2u(o.w))};
        __builtin_amdgcn_raw_buffer_store_b128(ov, rsW, pos, rec, AUX_SC1);
        if (w == 0 && ln < 16 && (ln & 3) == 0)
          __builtin_amdgcn_raw_buffer_store_b64(v2u{canon(f2u(M)), canon(f2u(L))}, rsW, 1024 + (h & 1) * 8 + (h >> 1) * (32 * PA_REC), rec, AUX_SC1);
      }
    }

    {
      if (w >= 2) __builtin_amdgcn_s_sleep(8);
      if (DLY_A) __builtin_amdgcn_s_sleep(DLY_A);
      v4u pa[8];
      v2u mlv;
      const int base = OFF_PA + (layer * 16 + rg) * 32 * PA_REC;
      CLOBBER();
      mlv = __builtin_amdgcn_raw_buffer_load_b64(rsW, 1024 + (ln >> 1) * PA_REC + (ln & 1) * 8, base, AUX_SC1);
#pragma unroll
      for (int k = 0; k < 8; ++k) pa[k] = ld128<AUX_SC1>(rsW, ln * 16, base + (8 * w + k) * PA_REC);
      SCHED();
      int rxo = ((8 * (ln & 3) + 4 * (ln >> 5) + 2 * ((ln >> 4) & 1)) * 4 + w) * 4, ppo = (16 * w + (ln >> 2)) * 16;
      asm volatile("" : "+v"(rxo), "+v"(ppo));
      if constexpr (layer == 1) {
#pragma unroll
        for (int i = 0; i < 4; ++i) wot[i] = ldsWq[i * 256 + w * 64 + ln];
      }
      f32x2 wof[4][4];
#pragma unroll
      for (int r = 0; r < 4; ++r)
#pragma unroll
        for (int i = 0; i < 4; ++i) { if (i == 0) asm volatile("" : "+v"(wot[r])); wof[r][i] = f32x2{bflo(wot[r][i]), bfhi(wot[r][i])}; asm volatile("" : "+v"(wof[r][i])); }
      SCHED();
_Pragma("unroll 1")
      for (int spin = 0; spin < SPIN_CAP; ++spin) {
        u32 mk[8], mxw = umax(mlv.x, mlv.y);
#pragma unroll
        for (int k = 0; k < 8; ++k) { mk[k] = max4(pa[k]); mxw = umax(mxw, mk[k]); }
        if (__all(mxw != SENT)) break;
        u32 need = __any(umax(mlv.x, mlv.y) == SENT) ? 0x100u : 0u;
#pragma unroll
        for (int k = 0; k < 8; ++k) need |= __any(mk[k] == SENT) ? (1u << k) : 0u;
        CLOBBER();
        if (need & 0x100) mlv = __builtin_amdgcn_raw_buffer_load_b64(rsW, 1024 + (ln >> 1) * PA_REC + (ln & 1) * 8, base, AUX_SC1);
#pragma unroll
        for (int k = 0; k < 8; ++k)
          if (need & (1 << k)) pa[k] = ld128<AUX_SC1>(rsW, ln * 16, base + (8 * w + k) * PA_REC);
        SCHED();
      }
      __builtin_amdgcn_s_waitcnt(0x0F70);
      if (sp == 0 && w == 0 && ln == 0) __builtin_amdgcn_raw_buffer_store_b32(1u, rsW, 0, OFF_FL + (layer * 8 + kvh) * 128, AUX_SC1);
      const float mm = u2f(mlv.x);
      float M = fmaxf(mm, dppf<XOR2>(mm));
      M = fmaxf(M, dppf<0x124>(M));
      M = fmaxf(M, dppf<ROR8>(M));
      M = xrow_max(M);
      const float wgt = __builtin_amdgcn_exp2f(mm - M);
      float L = wgt * u2f(mlv.y);
      L += dppf<XOR2>(L);
      L += dppf<0x124>(L);
      L += dppf<ROR8>(L);
      L = xrow_sum(L);
      const float fac = wgt * __builtin_amdgcn_rcpf(L);
      f32x4 o = {0.f, 0.f, 0.f, 0.f};
      const int src = (16 * w + ((ln >> 2) & 1)) * 4;
      float fk[8];
#pragma unroll
      for (int k = 0; k < 8; ++k) fk[k] = u2f((u32)__builtin_amdgcn_ds_bpermute(src + 8 * k, (int)f2u(fac)));
      SCHED();
#pragma unroll
      for (int k = 0; k < 8; ++k) o += fk[k] * __builtin_bit_cast(f32x4, pa[k]);
      f32x4* op = (f32x4*)(smem + L_MRG);
      op[w * 128 + ln] = o;
      BAR();
      const f32x4* opp = (const f32x4*)(smem + L_MRG + ppo);
      o = (opp[0] + opp[128]) + (opp[256] + opp[384]);
      float a[8];
#pragma unroll
      for (int i = 0; i < 4; ++i) {
        const f32x2 t = (o.x * wof[0][i] + o.y * wof[1][i]) + (o.z * wof[2][i] + o.w * wof[3][i]);
        a[2 * i] = t.x; a[2 * i + 1] = t.y;
      }
#pragma unroll
      for (int i = 0; i < 4; ++i) a[i] = swap32_add(a[i], a[i + 4]);
#pragma unroll
      for (int i = 0; i < 2; ++i) {
        a[i] = swap16_add(a[i], a[i + 2]);
        a[i] += dppf<ROR8>(a[i]);
        a[i] += dppf<0x124>(a[i]);
      }
      float* const rx = (float*)(smem + L_RX);
      if ((ln & 12) == 0) {
        float* const rp = (float*)(smem + L_RX + rxo);
        rp[0] = a[0];
        rp[4] = a[1];
      }
      BAR();
      if (w == 0 && ln < 32)
        __builtin_amdgcn_raw_buffer_store_b32(canon(f2u(sum4(*(const f32x4*)(rx + ln * 4)))), rsW, ln * 4, OFF_XB + (layer * 16 + rg) * 2048 + cs * 128, AUX_SC1);
    }

    {
      v4u wt[4];
#pragma unroll
      for (int i = 0; i < 4; ++i) wt[i] = ldsW13[i * 256 + w * 64 + ln];
      f32x2 wf[2][8];
      unpack_w(wt, wf);
      float cs13 = sum4(*(const f32x4*)(red + 5 * RED_STRIDE + (ln & 15) * 4));
      f32x2 xi = ((const f32x2*)(smem + L_XI))[w * 64 + ln];
      const int ro = red_slot(w, ln);
      int own = w * 128 + 4 * (ln & 31) + 2 * (ln >> 5) - dcol;
      asm volatile("" : "+v"(cs13), "+v"(xi), "+v"(own));
_Pragma("unroll 1")
      for (int spin = 0; spin < SPIN_CAP; ++spin) {
        CLOBBER();
        const u32 fl = __builtin_amdgcn_raw_buffer_load_b32(rsW, (ln & 7) * 128, OFF_FL + layer * 1024, AUX_SC1);
        if (__all(fl != SENT || (ln & 7) == kvh)) break;
      }
      v4u g[8];
_Pragma("unroll 1")
      for (int spin = 0; spin < SPIN_CAP; ++spin) {
        CLOBBER();
#pragma unroll
        for (int k = 0; k < 8; ++k) g[k] = ld128<AUX_SC1>(rsW, (ln & 31) * 16 + (ln >> 5) * 2048, OFF_XB + layer * 32768 + w * 512 + k * 4096);
        u32 mxw = 0;
#pragma unroll
        for (int k = 0; k < 8; ++k) mxw = umax(mxw, max4(g[k]));
        if (__all(mxw != SENT)) break;
        __builtin_amdgcn_s_sleep(DLY_RETRY);
      }
      const f32x4 sm = ((__builtin_bit_cast(f32x4, g[0]) + __builtin_bit_cast(f32x4, g[1])) + (__builtin_bit_cast(f32x4, g[2]) + __builtin_bit_cast(f32x4, g[3]))) +
                       ((__builtin_bit_cast(f32x4, g[4]) + __builtin_bit_cast(f32x4, g[5])) + (__builtin_bit_cast(f32x4, g[6]) + __builtin_bit_cast(f32x4, g[7])));
      const float x1a = xi.x + swap32_add(sm.x, sm.z), x1b = xi.y + swap32_add(sm.y, sm.w);
      if (own == 0) *(f32x2*)(red + 3 * RED_STRIDE) = f32x2{x1a, x1b};
      ln_gemv_part(x1a - cmean, x1b - cmean, wf, red + RED_STRIDE, ro, ln);
      BAR();
      float mu;
      const float gt = ln_gemv_final(red + RED_STRIDE, cs13, ln, mu);
      const float up = dppf<ROR8>(gt);
      const float hval = gt * __builtin_amdgcn_rcpf(1.0f + __expf(-gt)) * up;
      if (doS3 && w == 0 && ln < 8) __builtin_amdgcn_raw_buffer_store_b32(canon(f2u(hval)), rsW, ln * 4, OFF_H + layer * 8192 + s3 * 32, AUX_SC1);
      cmean = u2f((u32)__builtin_amdgcn_readfirstlane((int)f2u(cmean + mu)));
    }

    {
      f32x2 w2f[7];
#pragma unroll
      for (int k = 0; k < 7; ++k) {
        u32 t;
        if (k < 4) t = ((const u32*)(smem + L_W2))[k * 256 + w * 64 + ln];
        else { asm volatile("" : "+v"(w2r[k])); t = w2r[k]; }
        w2f[k] = f32x2{bflo(t), bfhi(t)};
        asm volatile("" : "+v"(w2f[k]));
      }
      if constexpr (layer == 0) { SCHED(); static_for<0, 4>(issue2); SCHED(); }
      __builtin_amdgcn_s_sleep(DLY_C1);
      u32 hv[7];
_Pragma("unroll 1")
      for (int spin = 0; spin < SPIN_CAP; ++spin) {
        CLOBBER();
        u32 mxw = 0;
#pragma unroll
        for (int k = 0; k < 7; ++k) hv[k] = __builtin_amdgcn_raw_buffer_load_b32(rsW, ln * 4, OFF_H + layer * 8192 + w * 256 + k * 1024, AUX_SC1);
#pragma unroll
        for (int k = 0; k < 7; ++k) mxw = umax(mxw, hv[k]);
        if (__all(mxw != SENT)) break;
        __builtin_amdgcn_s_sleep(DLY_RETRY);
      }
      f32x2 a = u2f(hv[0]) * w2f[0];
#pragma unroll
      for (int k = 1; k < 7; ++k) a += u2f(hv[k]) * w2f[k];
      float r = swap32_add(a.x, a.y);
      r = swap16_add(r, r);
      r += dppf<XOR1>(r); r += dppf<XOR2>(r); r += dppf<HMIR>(r); r += dppf<MIR>(r);
      float* const rd = red + 2 * RED_STRIDE;
      if ((ln & 31) == 0) rd[(ln >> 5) * 4 + w] = r;
      BAR();
      if (w == 0 && ln == 0) {
        const f32x2 x1 = *(const f32x2*)(red + 3 * RED_STRIDE);
        const float y0 = x1.x + sum4(*(const f32x4*)rd), y1 = x1.y + sum4(*(const f32x4*)(rd + 4));
        if (layer == 0) __builtin_amdgcn_raw_buffer_store_b64(v2u{canon(f2u(y0)), canon(f2u(y1))}, rsW, 0, OFF_X2 + dcol * 4, AUX_SC1);
        else *(u32*)(p.out + dcol) = pack_bf16(y0, y1);
      }
    }
    if constexpr (layer == 0) {

    {
      v4u wt[4];
#pragma unroll
      for (int i = 0; i < 4; ++i) wt[i] = ldsWq[i * 256 + w * 64 + ln];
      f32x2 wf[2][8];
      unpack_w(wt, wf);
      float csq = sum4(*(const f32x4*)(red + 4 * RED_STRIDE + (ln & 15) * 4));
      asm volatile("" : "+v"(csq));
#pragma unroll
      for (int i = 0; i < 4; ++i) ldsWq[i * 256 + w * 64 + ln] = wot[i];
      const int ro = red_slot(w, ln);
      SCHED(); static_for<4, 8>(issue2); SCHED();
      __builtin_amdgcn_s_sleep(DLY_C2);
      v2u g;
_Pragma("unroll 1")
      for (int spin = 0; spin < SPIN_CAP; ++spin) {
        CLOBBER();
        g = __builtin_amdgcn_raw_buffer_load_b64(rsW, (ln & 31) * 16 + (ln >> 5) * 8, OFF_X2 + w * 512, AUX_SC1);
        if (__all(umax(g.x, g.y) != SENT)) break;
        __builtin_amdgcn_s_sleep(DLY_RETRY);
      }
      const float xia = u2f(g.x), xib = u2f(g.y);
      ((v2u*)(smem + L_XI))[w * 64 + ln] = g;
      ln_gemv_part(xia - cmean, xib - cmean, wf, red, ro, ln);
      BAR();
      float mu;
      const float qv = ln_gemv_final(red, csq, ln, mu);
      const u32 pk = canon(pack_bf16(qv, dppf<XOR1>(qv)));
      if (w == 0 && ln < 16 && (ln & 1) == 0) __builtin_amdgcn_raw_buffer_store_b32(pk, rsW, ln * 2, OFF_Q1 + qcol * 8, AUX_SC1);
      cmean = u2f((u32)__builtin_amdgcn_readfirstlane((int)f2u(cmean + mu)));
    }
    int qo = voffK;
    asm volatile("" : "+v"(qo));
    qo = qoff(qo);
    SCHED(); static_for<8, NPRE>(issue2); SCHED();
    __builtin_amdgcn_s_sleep(DLY_D);
_Pragma("unroll 1")
    for (int spin = 0; spin < SPIN_CAP; ++spin) {
      CLOBBER();
      qf = ld128<AUX_SC1>(rsW, qo, OFF_Q1 + kvh * 4096);
      if (__all(max4(qf) != SENT)) break;
    }
    }
  };
  layer_body(IC<0>{});
  layer_body(IC<1>{});
  asm volatile(".Ldk_code_end:");
}

extern "C" void kernel_launch(void* const* d_in, const int* in_sizes, int n_in, void* d_out, int out_size,
                              void* d_ws, size_t ws_size, hipStream_t stream) {
  Params p;
  p.x = (const u16*)d_in[0];
  p.K = (const u16*)d_in[1];
  p.V = (const u16*)d_in[2];
  p.wq = (const u16*)d_in[3];
  p.wo = (const u16*)d_in[4];
  p.w1 = (const u16*)d_in[6];
  p.w3 = (const u16*)d_in[7];
  p.w2 = (const u16*)d_in[8];
  p.out = (u16*)d_out;
  p.ws = (unsigned char*)d_ws;
  decode_kernel<<<256, 256, 0, stream>>>(p);
}
```
